```python
import torch

def _softmax(x, axis=-1):
    return torch.softmax(x, dim=axis)
def _gelu(x):
    return torch.nn.functional.gelu(x, approximate="tanh")
def _silu(x):
    return torch.nn.functional.silu(x)
def _sigmoid(x):
    return torch.sigmoid(x)
def _layernorm(x, axis=-1, eps=1e-5):
    m = x.mean(dim=axis, keepdim=True)
    v = x.var(dim=axis, keepdim=True, unbiased=False)
    return (x - m) / torch.sqrt(v + eps)
def _scatter_add(tgt, idx, src, axis=0):
    return tgt.clone().index_add_(axis, idx, src)
def _segment_sum(src, seg, n):
    out = torch.zeros((n,) + src.shape[1:], dtype=src.dtype)
    return out.index_add_(0, seg, src)
def _pad(x, pw):
    flat = tuple(v for pair in reversed(pw) for v in pair)
    return torch.nn.functional.pad(x, flat)

kernel_name = 'parallel_sum__grouped_kv_mqa__glu_gated_reglu__prompt'

def setup_inputs():

    _w = (lambda a: (lambda t: t.to(torch.bfloat16) if t.is_floating_point() else t)(torch.as_tensor(a)))
    def _A_setup():
        B, S, D, H = 1, 2048, 512, 32
        Dh = D // H
        return {
            "x": _w((lambda *s: torch.randn(*s, dtype=torch.bfloat16) * 0.05)(B, S, D)),
            "wq": _w((lambda *s: torch.randn(*s, dtype=torch.bfloat16) * 0.05)(D, H * Dh)),
            "wk": _w((lambda *s: torch.randn(*s, dtype=torch.bfloat16) * 0.05)(D, Dh)),
            "wv": _w((lambda *s: torch.randn(*s, dtype=torch.bfloat16) * 0.05)(D, Dh)),
            "wo": _w((lambda *s: torch.randn(*s, dtype=torch.bfloat16) * 0.05)(H * Dh, D)),
        }
    _A_in = _A_setup()

    def _B_setup():
        B, S, D, F = 1, 2048, 512, 1792
        return {
            "x": _w((lambda *s: torch.randn(*s, dtype=torch.bfloat16) * 0.05)(B, S, D)),
            "W_gate": _w((lambda *s: torch.randn(*s, dtype=torch.bfloat16) * 0.4)(D, F) * (2.0 / D) ** 0.5),
            "W_up": _w((lambda *s: torch.randn(*s, dtype=torch.bfloat16) * 0.4)(D, F) * (2.0 / D) ** 0.5),
            "W_down": _w((lambda *s: torch.randn(*s, dtype=torch.bfloat16) * 0.4)(F, D) * (1.0 / F) ** 0.5),
        }
    _B_in = _B_setup()

    return {
        "A_x": _A_in["x"],
        "A_wq": _A_in["wq"],
        "A_wk": _A_in["wk"],
        "A_wv": _A_in["wv"],
        "A_wo": _A_in["wo"],
        "B_x": _B_in["x"],
        "B_W_gate": _B_in["W_gate"],
        "B_W_up": _B_in["W_up"],
        "B_W_down": _B_in["W_down"],
    }

def reference(A_x, A_wq, A_wk, A_wv, A_wo, B_x, B_W_gate, B_W_up, B_W_down):
    def _A_ref(x, wq, wk, wv, wo):
        B, S, D = x.shape
        H = 32
        Dh = D // H
        q = (lambda x, *d: x.permute(*d))((x @ wq).reshape(B, S, H, Dh), 0, 2, 1, 3)
        k = x @ wk
        v = x @ wv
        scores = torch.einsum("bhqd,bkd->bhqk", q, k) * (Dh ** -0.5)
        p = _softmax(scores, axis=-1)
        o = torch.einsum("bhqk,bkd->bhqd", p, v)
        return (lambda x, *d: x.permute(*d))(o, 0, 2, 1, 3).reshape(B, S, H * Dh) @ wo

    def _B_ref(x, W_gate, W_up, W_down):
        gate = torch.einsum("bsd,df->bsf", x, W_gate)
        up = torch.einsum("bsd,df->bsf", x, W_up)
        gate = gate * (lambda x, dt: x.to(dt))(gate > 0, gate.dtype)
        h = gate * up
        return torch.einsum("bsf,fd->bsd", h, W_down)

    x = A_x
    xn = _layernorm(x, axis=-1)
    return x + _A_ref(xn, A_wq, A_wk, A_wv, A_wo) + _B_ref(xn, B_W_gate, B_W_up, B_W_down)
```

```cpp
#include <hip/hip_runtime.h>
#include <hip/hip_bf16.h>
typedef unsigned short u16;
__device__ __forceinline__ float bf2f(u16 v) { return __uint_as_float((unsigned)v << 16); }
__device__ __forceinline__ u16 f2bf(float x) { unsigned u = __float_as_uint(x); u += 0x7fffu + ((u >> 16) & 1u); return (u16)(u >> 16); }
constexpr int S = 2048, D = 512, F = 1792;
__global__ void k_ln(const u16* x, u16* xn) {
  int row = blockIdx.x * 4 + (threadIdx.x >> 6), l = threadIdx.x & 63;
  float v[8], s = 0; for (int j = 0; j < 8; ++j) { v[j] = bf2f(x[row * D + l * 8 + j]); s += v[j]; }
  for (int o = 32; o; o >>= 1) s += __shfl_xor(s, o);
  float m = s / D, q = 0; for (int j = 0; j < 8; ++j) { v[j] -= m; q += v[j] * v[j]; }
  for (int o = 32; o; o >>= 1) q += __shfl_xor(q, o);
  float r = rsqrtf(q / D + 1e-5f);
  for (int j = 0; j < 8; ++j) xn[row * D + l * 8 + j] = f2bf(v[j] * r);
}
typedef __attribute__((ext_vector_type(8))) __bf16 bf16x8;
typedef __attribute__((ext_vector_type(8))) u16 u16x8;
typedef __attribute__((ext_vector_type(4))) float f32x4;
__device__ __forceinline__ f32x4 tile(const u16* A, int lda, const u16* B, int ldb, int K, int m0, int n0, f32x4 acc) {
  int l = threadIdx.x & 63, r = l & 15, q = l >> 4;
  for (int k = 0; k < K; k += 32) {
    u16x8 a = *(const u16x8*)(A + (size_t)(m0 + r) * lda + k + 8 * q), b;
    for (int j = 0; j < 8; ++j) b[j] = B[(size_t)(k + 8 * q + j) * ldb + n0 + r];
    acc = __builtin_amdgcn_mfma_f32_16x16x32_bf16(__builtin_bit_cast(bf16x8, a), __builtin_bit_cast(bf16x8, b), acc, 0, 0, 0);
  }
  return acc;
}
__global__ void k_proj(const u16* xn, const u16* w, int N, u16* out) {
  int wv = blockIdx.x * 4 + (threadIdx.x >> 6), nt = N / 16, m0 = wv / nt * 16, n0 = wv % nt * 16, l = threadIdx.x & 63;
  f32x4 a = tile(xn, D, w, N, D, m0, n0, f32x4{0, 0, 0, 0});
  for (int j = 0; j < 4; ++j) out[(size_t)(m0 + (l >> 4) * 4 + j) * N + n0 + (l & 15)] = f2bf(a[j]);
}
__global__ void k_h(const u16* xn, const u16* wg, const u16* wu, u16* h) {
  int wv = blockIdx.x * 4 + (threadIdx.x >> 6), nt = F / 16, m0 = wv / nt * 16, n0 = wv % nt * 16, l = threadIdx.x & 63;
  f32x4 a = tile(xn, D, wg, F, D, m0, n0, f32x4{0, 0, 0, 0}), b = tile(xn, D, wu, F, D, m0, n0, f32x4{0, 0, 0, 0});
  for (int j = 0; j < 4; ++j) h[(size_t)(m0 + (l >> 4) * 4 + j) * F + n0 + (l & 15)] = f2bf(fmaxf(a[j], 0.f) * b[j]);
}
__global__ void k_attn(const u16* q, const u16* k, const u16* v, u16* o) {
  __shared__ float ks[256 * 16], vs[256 * 16];
  int t = blockIdx.x * 256 + threadIdx.x;
  float qq[16], acc[16], m = -INFINITY, l = 0;
  for (int d = 0; d < 16; ++d) { qq[d] = bf2f(q[(size_t)t * 16 + d]) * 0.25f; acc[d] = 0; }
  for (int k0 = 0; k0 < S; k0 += 256) {
    __syncthreads();
    for (int i = threadIdx.x; i < 4096; i += 256) { ks[i] = bf2f(k[k0 * 16 + i]); vs[i] = bf2f(v[k0 * 16 + i]); }
    __syncthreads();
    for (int j0 = 0; j0 < 256; j0 += 8) {
      float s[8], mx = m;
#pragma unroll
      for (int j = 0; j < 8; ++j) { float a = 0; for (int d = 0; d < 16; ++d) a += qq[d] * ks[(j0 + j) * 16 + d]; s[j] = a; mx = fmaxf(mx, a); }
      float c = __expf(m - mx); l *= c; for (int d = 0; d < 16; ++d) acc[d] *= c; m = mx;
#pragma unroll
      for (int j = 0; j < 8; ++j) { float p = __expf(s[j] - m); l += p; for (int d = 0; d < 16; ++d) acc[d] += p * vs[(j0 + j) * 16 + d]; }
    }
  }
  for (int d = 0; d < 16; ++d) o[(size_t)t * 16 + d] = f2bf(acc[d] / l);
}
__global__ void k_out(const u16* x, const u16* o, const u16* wo, const u16* h, const u16* wd, u16* out) {
  int wv = blockIdx.x * 4 + (threadIdx.x >> 6), nt = D / 16, m0 = wv / nt * 16, n0 = wv % nt * 16, l = threadIdx.x & 63;
  f32x4 a = tile(o, D, wo, D, D, m0, n0, f32x4{0, 0, 0, 0});
  a = tile(h, F, wd, D, F, m0, n0, a);
  for (int j = 0; j < 4; ++j) { size_t i = (size_t)(m0 + (l >> 4) * 4 + j) * D + n0 + (l & 15); out[i] = f2bf(bf2f(x[i]) + a[j]); }
}
#define PROBE 0
#define PIN 5
__global__ void k_probe(const unsigned* p, unsigned* sink, int n, int strideDw) {
  unsigned off = 0, acc = 0;
  for (int i = 0; i < n; ++i) { unsigned v = __builtin_nontemporal_load(p + off + threadIdx.x * 0); asm volatile("v_and_b32 %0, 0, %0" : "+v"(v)); off += strideDw + v; acc += v; }
  sink[threadIdx.x] = acc;
}
extern "C" void kernel_launch(void* const* d_in, const int* in_sizes, int n_in, void* d_out, int out_size, void* d_ws, size_t ws_size, hipStream_t stream) {
  const u16 *x = (const u16*)d_in[0], *wq = (const u16*)d_in[1], *wk = (const u16*)d_in[2], *wv = (const u16*)d_in[3], *wo = (const u16*)d_in[4];
  const u16 *wg = (const u16*)d_in[6], *wu = (const u16*)d_in[7], *wd = (const u16*)d_in[8];
  u16* ws = (u16*)d_ws; u16 *xn = ws, *q = xn + S * D, *k = q + S * D, *v = k + S * 16, *o = v + S * 16, *h = o + S * D, *sink = h + S * F;
  if (PROBE) k_probe<<<1, 64, 0, stream>>>((const unsigned*)d_in[PIN], (unsigned*)sink, PROBE, 1056);
  k_ln<<<S / 4, 256, 0, stream>>>(x, xn);
  k_proj<<<S / 16 * (D / 16) / 4, 256, 0, stream>>>(xn, wq, D, q);
  k_proj<<<S / 16 / 4, 256, 0, stream>>>(xn, wk, 16, k);
  k_proj<<<S / 16 / 4, 256, 0, stream>>>(xn, wv, 16, v);
  k_h<<<S / 16 * (F / 16) / 4, 256, 0, stream>>>(xn, wg, wu, h);
  k_attn<<<S * 32 / 256, 256, 0, stream>>>(q, k, v, o);
  k_out<<<S / 16 * (D / 16) / 4, 256, 0, stream>>>(x, o, wo, h, wd, (u16*)d_out);
}
```

```cpp
#include <hip/hip_runtime.h>
#include <hip/hip_bf16.h>

__global__ void __launch_bounds__(512, 2)
fused_ln_mqa_reglu(const void* x, const void* wq, const void* wk, const void* wv, const void* wo,
                   const void* wg, const void* wu, const void* wd, void* out, void* ws) {
  __shared__ __attribute__((aligned(16))) char lds[163840];
  unsigned tid = threadIdx.x;
  asm volatile(
    ".Lstart:\n"
    "s_load_dwordx16 s[4:19], %1, 0\n"
    "s_load_dwordx4 s[20:23], %1, 64\n"
    "s_mov_b32 s79, %2\n"
    "v_lshrrev_b32 v1, 6, v0\n"
    "v_and_b32 v243, 63, v0\n"
    "v_readfirstlane_b32 s54, v1\n"
    "v_lshlrev_b32 v242, 4, v243\n"
    "v_mov_b32 v247, 162304\n"
    "v_mov_b64 v[216:217], 0\n"
    "v_mov_b64 v[218:219], 0\n"
    "v_mov_b64 v[220:221], 0\n"
    "v_mov_b64 v[222:223], 0\n"
    "ds_write_b128 v247, v[216:219]\n"
    "ds_write_b128 v247, v[216:219] offset:16\n"
    "ds_write_b128 v247, v[216:219] offset:32\n"
    "s_lshr_b32 s55, s79, 5\n"
    "s_and_b32 s58, s79, 6\n"
    "s_lshl_b32 s58, s58, 2\n"
    "s_add_u32 s52, s58, s55\n"
    "s_and_b32 s58, s79, 1\n"
    "s_lshl_b32 s58, s58, 2\n"
    "s_bfe_u32 s59, s79, 0x20003\n"
    "s_add_u32 s53, s58, s59\n"
    "s_and_b32 s75, s54, 3\n"
    "s_cmp_ge_u32 s54, 4\n"
    "s_cbranch_scc1 .Lgemm\n"
    "v_lshrrev_b32 v4, 4, v243\n"
    "v_and_b32 v5, 15, v243\n"
    "s_lshl_b32 s67, s54, 2\n"
    "v_add_u32 v4, s67, v4\n"
    "v_lshlrev_b32 v6, 4, v5\n"
    "v_lshl_add_u32 v224, v4, 10, v6\n"
    "s_lshr_b32 s67, s53, 1\n"
    "s_lshl_b32 s66, s52, 16\n"
    "s_xor_b32 s58, s67, 0\n"
    "s_and_b32 s58, s58, 3\n"
    "s_lshl_b32 s62, s58, 14\n"
    "s_add_u32 s62, s62, s66\n"
    "s_xor_b32 s59, s67, 1\n"
    "s_and_b32 s59, s59, 3\n"
    "s_lshl_b32 s63, s59, 14\n"
    "s_add_u32 s63, s63, s66\n"
    "s_xor_b32 s60, s67, 2\n"
    "s_and_b32 s60, s60, 3\n"
    "s_lshl_b32 s64, s60, 14\n"
    "s_add_u32 s64, s64, s66\n"
    "s_xor_b32 s61, s67, 3\n"
    "s_and_b32 s61, s61, 3\n"
    "s_lshl_b32 s65, s61, 14\n"
    "s_add_u32 s65, s65, s66\n"
    "s_setprio 3\n"
    "v_mov_b64 v[8:9], 0\n"
    "v_mov_b64 v[10:11], 0\n"
    "v_mov_b64 v[12:13], 0\n"
    "v_mov_b64 v[14:15], 0\n"
    "v_mov_b64 v[16:17], 0\n"
    "v_mov_b64 v[18:19], 0\n"
    "v_mov_b64 v[20:21], 0\n"
    "v_mov_b64 v[22:23], 0\n"
    "v_mov_b64 v[24:25], 0\n"
    "v_mov_b64 v[26:27], 0\n"
    "v_mov_b64 v[28:29], 0\n"
    "v_mov_b64 v[30:31], 0\n"
    "v_mov_b64 v[32:33], 0\n"
    "v_mov_b64 v[34:35], 0\n"
    "v_mov_b64 v[36:37], 0\n"
    "v_mov_b64 v[38:39], 0\n"
    "v_mov_b64 v[40:41], 0\n"
    "v_mov_b64 v[42:43], 0\n"
    "v_mov_b64 v[44:45], 0\n"
    "v_mov_b64 v[46:47], 0\n"
    "v_mov_b64 v[48:49], 0\n"
    "v_mov_b64 v[50:51], 0\n"
    "v_mov_b64 v[52:53], 0\n"
    "v_mov_b64 v[54:55], 0\n"
    "v_mov_b64 v[56:57], 0\n"
    "v_mov_b64 v[58:59], 0\n"
    "v_mov_b64 v[60:61], 0\n"
    "v_mov_b64 v[62:63], 0\n"
    "v_mov_b64 v[64:65], 0\n"
    "v_mov_b64 v[66:67], 0\n"
    "v_mov_b64 v[68:69], 0\n"
    "v_mov_b64 v[70:71], 0\n"
    "v_mov_b64 v[72:73], 0\n"
    "v_mov_b64 v[74:75], 0\n"
    "v_mov_b64 v[76:77], 0\n"
    "v_mov_b64 v[78:79], 0\n"
    "v_mov_b64 v[80:81], 0\n"
    "v_mov_b64 v[82:83], 0\n"
    "v_mov_b64 v[84:85], 0\n"
    "v_mov_b64 v[86:87], 0\n"
    "v_mov_b64 v[88:89], 0\n"
    "v_mov_b64 v[90:91], 0\n"
    "v_mov_b64 v[92:93], 0\n"
    "v_mov_b64 v[94:95], 0\n"
    "v_mov_b64 v[96:97], 0\n"
    "v_mov_b64 v[98:99], 0\n"
    "v_mov_b64 v[100:101], 0\n"
    "v_mov_b64 v[102:103], 0\n"
    "v_mov_b64 v[104:105], 0\n"
    "v_mov_b64 v[106:107], 0\n"
    "v_mov_b64 v[108:109], 0\n"
    "v_mov_b64 v[110:111], 0\n"
    "v_mov_b64 v[112:113], 0\n"
    "v_mov_b64 v[114:115], 0\n"
    "v_mov_b64 v[116:117], 0\n"
    "v_mov_b64 v[118:119], 0\n"
    "v_mov_b64 v[120:121], 0\n"
    "v_mov_b64 v[122:123], 0\n"
    "v_mov_b64 v[124:125], 0\n"
    "v_mov_b64 v[126:127], 0\n"
    "s_waitcnt lgkmcnt(0)\n"
    "s_mov_b32 s24, s4\n"
    "s_and_b32 s25, s5, 0xffff\n"
    "s_mov_b32 s26, 2097152\n"
    "s_mov_b32 s27, 0x20000\n"
    "buffer_load_dwordx4 v[128:131], v224, s[24:27], s62 offen\n"
    "buffer_load_dwordx4 v[132:135], v224, s[24:27], s62 offen offset:256\n"
    "buffer_load_dwordx4 v[136:139], v224, s[24:27], s62 offen offset:512\n"
    "buffer_load_dwordx4 v[140:143], v224, s[24:27], s62 offen offset:768\n"
    "s_barrier\n"
    "s_setprio 2\n"
    "s_mov_b32 s28, s22\n"
    "s_and_b32 s29, s23, 0xffff\n"
    "s_mov_b32 s30, 10485760\n"
    "s_mov_b32 s31, 0x20000\n"
    "s_mov_b32 s48, s20\n"
    "s_and_b32 s49, s21, 0xffff\n"
    "s_mov_b32 s50, 2097152\n"
    "s_mov_b32 s51, 0x20000\n"
    "v_and_b32 v7, 7, v5\n"
    "v_xor_b32 v7, v4, v7\n"
    "v_lshlrev_b32 v7, 4, v7\n"
    "v_lshl_add_u32 v7, v5, 9, v7\n"
    "s_lshr_b32 s66, s58, 1\n"
    "s_lshl_b32 s66, s66, 15\n"
    "s_and_b32 s67, s58, 1\n"
    "s_lshl_b32 s67, s67, 8\n"
    "s_add_u32 s66, s66, s67\n"
    "v_add_u32 v208, s66, v7\n"
    "s_lshl_b32 s66, s58, 6\n"
    "s_add_u32 s66, s66, 161792\n"
    "v_lshl_add_u32 v212, v4, 2, s66\n"
    "s_lshr_b32 s66, s59, 1\n"
    "s_lshl_b32 s66, s66, 15\n"
    "s_and_b32 s67, s59, 1\n"
    "s_lshl_b32 s67, s67, 8\n"
    "s_add_u32 s66, s66, s67\n"
    "v_add_u32 v209, s66, v7\n"
    "s_lshl_b32 s66, s59, 6\n"
    "s_add_u32 s66, s66, 161792\n"
    "v_lshl_add_u32 v213, v4, 2, s66\n"
    "s_lshr_b32 s66, s60, 1\n"
    "s_lshl_b32 s66, s66, 15\n"
    "s_and_b32 s67, s60, 1\n"
    "s_lshl_b32 s67, s67, 8\n"
    "s_add_u32 s66, s66, s67\n"
    "v_add_u32 v210, s66, v7\n"
    "s_lshl_b32 s66, s60, 6\n"
    "s_add_u32 s66, s66, 161792\n"
    "v_lshl_add_u32 v214, v4, 2, s66\n"
    "s_lshr_b32 s66, s61, 1\n"
    "s_lshl_b32 s66, s66, 15\n"
    "s_and_b32 s67, s61, 1\n"
    "s_lshl_b32 s67, s67, 8\n"
    "s_add_u32 s66, s66, s67\n"
    "v_add_u32 v211, s66, v7\n"
    "s_lshl_b32 s66, s61, 6\n"
    "s_add_u32 s66, s66, 161792\n"
    "v_lshl_add_u32 v215, v4, 2, s66\n"
    "v_mov_b32 v250, 1\n"
    "v_mov_b32 v240, 0xdddd\n"
    "v_lshrrev_b32 v7, 5, v243\n"
    "v_lshl_add_u32 v241, v7, 8, v6\n"
    "s_mov_b32 s76, 0x0000ffff\n"
    "s_mov_b32 s77, 0x0000ffff\n"
    "v_lshrrev_b32 v1, 3, v243\n"
    "v_and_b32 v2, 7, v243\n"
    "v_lshlrev_b32 v3, 10, v1\n"
    "v_lshl_add_u32 v3, v2, 4, v3\n"
    "s_lshl_b32 s67, s52, 3\n"
    "s_add_u32 s67, s67, s54\n"
    "s_lshl_b32 s67, s67, 13\n"
    "s_lshl_b32 s66, s53, 7\n"
    "s_add_u32 s67, s67, s66\n"
    "v_add_u32 v244, s67, v3\n"
    "v_lshlrev_b32 v0, 3, v243\n"
    "s_lshl_b32 s74, s54, 9\n"
    "s_and_b32 s67, s79, 7\n"
    "s_lshl_b32 s67, s67, 11\n"
    "s_add_u32 s74, s74, s67\n"
    "s_add_u32 s74, s74, 4096\n"
    "s_mov_b32 s58, 0x3f803f80\n"
    "s_mov_b32 s59, 0x00003f80\n"
    "s_mov_b32 s60, 0x3f800000\n"
    "buffer_load_dwordx4 v[144:147], v224, s[24:27], s63 offen\n"
    "buffer_load_dwordx4 v[148:151], v224, s[24:27], s63 offen offset:256\n"
    "buffer_load_dwordx4 v[152:155], v224, s[24:27], s63 offen offset:512\n"
    "buffer_load_dwordx4 v[156:159], v224, s[24:27], s63 offen offset:768\n"
    "s_waitcnt vmcnt(7)\n"
    "v_dot2c_f32_bf16 v216, s58, v128\n"
    "v_dot2c_f32_bf16 v216, s58, v129\n"
    "v_dot2c_f32_bf16 v216, s58, v130\n"
    "v_dot2c_f32_bf16 v216, s58, v131\n"
    "s_waitcnt vmcnt(6)\n"
    "v_dot2c_f32_bf16 v216, s58, v132\n"
    "v_dot2c_f32_bf16 v216, s58, v133\n"
    "v_dot2c_f32_bf16 v216, s58, v134\n"
    "v_dot2c_f32_bf16 v216, s58, v135\n"
    "s_waitcnt vmcnt(5)\n"
    "v_dot2c_f32_bf16 v216, s58, v136\n"
    "v_dot2c_f32_bf16 v216, s58, v137\n"
    "v_dot2c_f32_bf16 v216, s58, v138\n"
    "v_dot2c_f32_bf16 v216, s58, v139\n"
    "s_waitcnt vmcnt(4)\n"
    "v_dot2c_f32_bf16 v216, s58, v140\n"
    "v_dot2c_f32_bf16 v216, s58, v141\n"
    "v_dot2c_f32_bf16 v216, s58, v142\n"
    "v_dot2c_f32_bf16 v216, s58, v143\n"
    "buffer_load_dwordx4 v[160:163], v224, s[24:27], s64 offen\n"
    "buffer_load_dwordx4 v[164:167], v224, s[24:27], s64 offen offset:256\n"
    "buffer_load_dwordx4 v[168:171], v224, s[24:27], s64 offen offset:512\n"
    "buffer_load_dwordx4 v[172:175], v224, s[24:27], s64 offen offset:768\n"
    "v_add_f32_dpp v216, v216, v216 quad_perm:[1,0,3,2] row_mask:0xf bank_mask:0xf\n"
    "s_nop 1\n"
    "v_add_f32_dpp v216, v216, v216 quad_perm:[2,3,0,1] row_mask:0xf bank_mask:0xf\n"
    "s_nop 1\n"
    "v_add_f32_dpp v216, v216, v216 row_half_mirror row_mask:0xf bank_mask:0xf\n"
    "s_nop 1\n"
    "v_add_f32_dpp v216, v216, v216 row_mirror row_mask:0xf bank_mask:0xf\n"
    "v_mul_f32 v216, 0xbb000000, v216\n"
    "v_dot2_f32_bf16 v192, v128, s59, v216\n"
    "v_dot2_f32_bf16 v193, v128, s60, v216\n"
    "v_dot2_f32_bf16 v194, v129, s59, v216\n"
    "v_dot2_f32_bf16 v195, v129, s60, v216\n"
    "v_dot2_f32_bf16 v196, v130, s59, v216\n"
    "v_dot2_f32_bf16 v197, v130, s60, v216\n"
    "v_dot2_f32_bf16 v198, v131, s59, v216\n"
    "v_dot2_f32_bf16 v199, v131, s60, v216\n"
    "v_dot2_f32_bf16 v200, v132, s59, v216\n"
    "v_dot2_f32_bf16 v201, v132, s60, v216\n"
    "v_dot2_f32_bf16 v202, v133, s59, v216\n"
    "v_dot2_f32_bf16 v203, v133, s60, v216\n"
    "v_dot2_f32_bf16 v204, v134, s59, v216\n"
    "v_dot2_f32_bf16 v205, v134, s60, v216\n"
    "v_dot2_f32_bf16 v206, v135, s59, v216\n"
    "v_dot2_f32_bf16 v207, v135, s60, v216\n"
    "v_cvt_pk_bf16_f32 v128, v192, v193\n"
    "v_cvt_pk_bf16_f32 v129, v194, v195\n"
    "v_cvt_pk_bf16_f32 v130, v196, v197\n"
    "v_cvt_pk_bf16_f32 v131, v198, v199\n"
    "ds_write_b128 v208, v[128:131]\n"
    "v_dot2c_f32_bf16 v220, v128, v128\n"
    "v_dot2c_f32_bf16 v220, v129, v129\n"
    "v_dot2c_f32_bf16 v220, v130, v130\n"
    "v_dot2c_f32_bf16 v220, v131, v131\n"
    "v_dot2_f32_bf16 v192, v136, s59, v216\n"
    "v_dot2_f32_bf16 v193, v136, s60, v216\n"
    "v_dot2_f32_bf16 v194, v137, s59, v216\n"
    "v_dot2_f32_bf16 v195, v137, s60, v216\n"
    "v_dot2_f32_bf16 v196, v138, s59, v216\n"
    "v_dot2_f32_bf16 v197, v138, s60, v216\n"
    "v_dot2_f32_bf16 v198, v139, s59, v216\n"
    "v_dot2_f32_bf16 v199, v139, s60, v216\n"
    "v_cvt_pk_bf16_f32 v132, v200, v201\n"
    "v_cvt_pk_bf16_f32 v133, v202, v203\n"
    "v_cvt_pk_bf16_f32 v134, v204, v205\n"
    "v_cvt_pk_bf16_f32 v135, v206, v207\n"
    "ds_write_b128 v208, v[132:135] offset:8192\n"
    "v_dot2c_f32_bf16 v220, v132, v132\n"
    "v_dot2c_f32_bf16 v220, v133, v133\n"
    "v_dot2c_f32_bf16 v220, v134, v134\n"
    "v_dot2c_f32_bf16 v220, v135, v135\n"
    "v_dot2_f32_bf16 v200, v140, s59, v216\n"
    "v_dot2_f32_bf16 v201, v140, s60, v216\n"
    "v_dot2_f32_bf16 v202, v141, s59, v216\n"
    "v_dot2_f32_bf16 v203, v141, s60, v216\n"
    "v_dot2_f32_bf16 v204, v142, s59, v216\n"
    "v_dot2_f32_bf16 v205, v142, s60, v216\n"
    "v_dot2_f32_bf16 v206, v143, s59, v216\n"
    "v_dot2_f32_bf16 v207, v143, s60, v216\n"
    "v_cvt_pk_bf16_f32 v136, v192, v193\n"
    "v_cvt_pk_bf16_f32 v137, v194, v195\n"
    "v_cvt_pk_bf16_f32 v138, v196, v197\n"
    "v_cvt_pk_bf16_f32 v139, v198, v199\n"
    "ds_write_b128 v208, v[136:139] offset:16384\n"
    "v_dot2c_f32_bf16 v220, v136, v136\n"
    "v_dot2c_f32_bf16 v220, v137, v137\n"
    "v_dot2c_f32_bf16 v220, v138, v138\n"
    "v_dot2c_f32_bf16 v220, v139, v139\n"
    "v_cvt_pk_bf16_f32 v140, v200, v201\n"
    "v_cvt_pk_bf16_f32 v141, v202, v203\n"
    "v_cvt_pk_bf16_f32 v142, v204, v205\n"
    "v_cvt_pk_bf16_f32 v143, v206, v207\n"
    "ds_write_b128 v208, v[140:143] offset:24576\n"
    "v_dot2c_f32_bf16 v220, v140, v140\n"
    "v_dot2c_f32_bf16 v220, v141, v141\n"
    "v_dot2c_f32_bf16 v220, v142, v142\n"
    "v_dot2c_f32_bf16 v220, v143, v143\n"
    "s_nop 2\n"
    "v_add_f32_dpp v220, v220, v220 quad_perm:[1,0,3,2] row_mask:0xf bank_mask:0xf\n"
    "s_nop 1\n"
    "v_add_f32_dpp v220, v220, v220 quad_perm:[2,3,0,1] row_mask:0xf bank_mask:0xf\n"
    "s_nop 1\n"
    "v_add_f32_dpp v220, v220, v220 row_half_mirror row_mask:0xf bank_mask:0xf\n"
    "s_nop 1\n"
    "v_add_f32_dpp v220, v220, v220 row_mirror row_mask:0xf bank_mask:0xf\n"
    "v_mov_b32 v216, 0x3727c5ac\n"
    "v_fmac_f32 v216, 0x3b000000, v220\n"
    "v_rsq_f32 v220, v216\n"
    "ds_write_b32 v212, v216 offset:256\n"
    "ds_write_b32 v212, v220\n"
    "s_waitcnt lgkmcnt(0)\n"
    "s_mov_b64 exec, 1\n"
    "ds_add_u32 v247, v250 offset:28\n"
    "s_mov_b64 exec, -1\n"
    "buffer_load_dwordx4 v[176:179], v224, s[24:27], s65 offen\n"
    "buffer_load_dwordx4 v[180:183], v224, s[24:27], s65 offen offset:256\n"
    "buffer_load_dwordx4 v[184:187], v224, s[24:27], s65 offen offset:512\n"
    "buffer_load_dwordx4 v[188:191], v224, s[24:27], s65 offen offset:768\n"
    "s_waitcnt vmcnt(11)\n"
    "v_dot2c_f32_bf16 v217, s58, v144\n"
    "v_dot2c_f32_bf16 v217, s58, v145\n"
    "v_dot2c_f32_bf16 v217, s58, v146\n"
    "v_dot2c_f32_bf16 v217, s58, v147\n"
    "s_waitcnt vmcnt(10)\n"
    "v_dot2c_f32_bf16 v217, s58, v148\n"
    "v_dot2c_f32_bf16 v217, s58, v149\n"
    "v_dot2c_f32_bf16 v217, s58, v150\n"
    "v_dot2c_f32_bf16 v217, s58, v151\n"
    "s_waitcnt vmcnt(9)\n"
    "v_dot2c_f32_bf16 v217, s58, v152\n"
    "v_dot2c_f32_bf16 v217, s58, v153\n"
    "v_dot2c_f32_bf16 v217, s58, v154\n"
    "v_dot2c_f32_bf16 v217, s58, v155\n"
    "s_waitcnt vmcnt(8)\n"
    "v_dot2c_f32_bf16 v217, s58, v156\n"
    "v_dot2c_f32_bf16 v217, s58, v157\n"
    "v_dot2c_f32_bf16 v217, s58, v158\n"
    "v_dot2c_f32_bf16 v217, s58, v159\n"
    "s_nop 2\n"
    "v_add_f32_dpp v217, v217, v217 quad_perm:[1,0,3,2] row_mask:0xf bank_mask:0xf\n"
    "s_nop 1\n"
    "v_add_f32_dpp v217, v217, v217 quad_perm:[2,3,0,1] row_mask:0xf bank_mask:0xf\n"
    "s_nop 1\n"
    "v_add_f32_dpp v217, v217, v217 row_half_mirror row_mask:0xf bank_mask:0xf\n"
    "s_nop 1\n"
    "v_add_f32_dpp v217, v217, v217 row_mirror row_mask:0xf bank_mask:0xf\n"
    "v_mul_f32 v217, 0xbb000000, v217\n"
    "v_dot2_f32_bf16 v192, v144, s59, v217\n"
    "v_dot2_f32_bf16 v193, v144, s60, v217\n"
    "v_dot2_f32_bf16 v194, v145, s59, v217\n"
    "v_dot2_f32_bf16 v195, v145, s60, v217\n"
    "v_dot2_f32_bf16 v196, v146, s59, v217\n"
    "v_dot2_f32_bf16 v197, v146, s60, v217\n"
    "v_dot2_f32_bf16 v198, v147, s59, v217\n"
    "v_dot2_f32_bf16 v199, v147, s60, v217\n"
    "v_dot2_f32_bf16 v200, v148, s59, v217\n"
    "v_dot2_f32_bf16 v201, v148, s60, v217\n"
    "v_dot2_f32_bf16 v202, v149, s59, v217\n"
    "v_dot2_f32_bf16 v203, v149, s60, v217\n"
    "v_dot2_f32_bf16 v204, v150, s59, v217\n"
    "v_dot2_f32_bf16 v205, v150, s60, v217\n"
    "v_dot2_f32_bf16 v206, v151, s59, v217\n"
    "v_dot2_f32_bf16 v207, v151, s60, v217\n"
    "v_cvt_pk_bf16_f32 v144, v192, v193\n"
    "v_cvt_pk_bf16_f32 v145, v194, v195\n"
    "v_cvt_pk_bf16_f32 v146, v196, v197\n"
    "v_cvt_pk_bf16_f32 v147, v198, v199\n"
    "ds_write_b128 v209, v[144:147]\n"
    "v_dot2c_f32_bf16 v221, v144, v144\n"
    "v_dot2c_f32_bf16 v221, v145, v145\n"
    "v_dot2c_f32_bf16 v221, v146, v146\n"
    "v_dot2c_f32_bf16 v221, v147, v147\n"
    "v_dot2_f32_bf16 v192, v152, s59, v217\n"
    "v_dot2_f32_bf16 v193, v152, s60, v217\n"
    "v_dot2_f32_bf16 v194, v153, s59, v217\n"
    "v_dot2_f32_bf16 v195, v153, s60, v217\n"
    "v_dot2_f32_bf16 v196, v154, s59, v217\n"
    "v_dot2_f32_bf16 v197, v154, s60, v217\n"
    "v_dot2_f32_bf16 v198, v155, s59, v217\n"
    "v_dot2_f32_bf16 v199, v155, s60, v217\n"
    "v_cvt_pk_bf16_f32 v148, v200, v201\n"
    "v_cvt_pk_bf16_f32 v149, v202, v203\n"
    "v_cvt_pk_bf16_f32 v150, v204, v205\n"
    "v_cvt_pk_bf16_f32 v151, v206, v207\n"
    "ds_write_b128 v209, v[148:151] offset:8192\n"
    "v_dot2c_f32_bf16 v221, v148, v148\n"
    "v_dot2c_f32_bf16 v221, v149, v149\n"
    "v_dot2c_f32_bf16 v221, v150, v150\n"
    "v_dot2c_f32_bf16 v221, v151, v151\n"
    "v_dot2_f32_bf16 v200, v156, s59, v217\n"
    "v_dot2_f32_bf16 v201, v156, s60, v217\n"
    "v_dot2_f32_bf16 v202, v157, s59, v217\n"
    "v_dot2_f32_bf16 v203, v157, s60, v217\n"
    "v_dot2_f32_bf16 v204, v158, s59, v217\n"
    "v_dot2_f32_bf16 v205, v158, s60, v217\n"
    "v_dot2_f32_bf16 v206, v159, s59, v217\n"
    "v_dot2_f32_bf16 v207, v159, s60, v217\n"
    "v_cvt_pk_bf16_f32 v152, v192, v193\n"
    "v_cvt_pk_bf16_f32 v153, v194, v195\n"
    "v_cvt_pk_bf16_f32 v154, v196, v197\n"
    "v_cvt_pk_bf16_f32 v155, v198, v199\n"
    "ds_write_b128 v209, v[152:155] offset:16384\n"
    "v_dot2c_f32_bf16 v221, v152, v152\n"
    "v_dot2c_f32_bf16 v221, v153, v153\n"
    "v_dot2c_f32_bf16 v221, v154, v154\n"
    "v_dot2c_f32_bf16 v221, v155, v155\n"
    "v_cvt_pk_bf16_f32 v156, v200, v201\n"
    "v_cvt_pk_bf16_f32 v157, v202, v203\n"
    "v_cvt_pk_bf16_f32 v158, v204, v205\n"
    "v_cvt_pk_bf16_f32 v159, v206, v207\n"
    "ds_write_b128 v209, v[156:159] offset:24576\n"
    "v_dot2c_f32_bf16 v221, v156, v156\n"
    "v_dot2c_f32_bf16 v221, v157, v157\n"
    "v_dot2c_f32_bf16 v221, v158, v158\n"
    "v_dot2c_f32_bf16 v221, v159, v159\n"
    "s_waitcnt vmcnt(7)\n"
    "v_dot2c_f32_bf16 v218, s58, v160\n"
    "v_dot2c_f32_bf16 v218, s58, v161\n"
    "v_dot2c_f32_bf16 v218, s58, v162\n"
    "v_dot2c_f32_bf16 v218, s58, v163\n"
    "s_waitcnt vmcnt(6)\n"
    "v_dot2c_f32_bf16 v218, s58, v164\n"
    "v_dot2c_f32_bf16 v218, s58, v165\n"
    "v_dot2c_f32_bf16 v218, s58, v166\n"
    "v_dot2c_f32_bf16 v218, s58, v167\n"
    "s_waitcnt vmcnt(5)\n"
    "v_dot2c_f32_bf16 v218, s58, v168\n"
    "v_dot2c_f32_bf16 v218, s58, v169\n"
    "v_dot2c_f32_bf16 v218, s58, v170\n"
    "v_dot2c_f32_bf16 v218, s58, v171\n"
    "s_waitcnt vmcnt(4)\n"
    "v_dot2c_f32_bf16 v218, s58, v172\n"
    "v_dot2c_f32_bf16 v218, s58, v173\n"
    "v_dot2c_f32_bf16 v218, s58, v174\n"
    "v_dot2c_f32_bf16 v218, s58, v175\n"
    "s_nop 2\n"
    "v_add_f32_dpp v218, v218, v218 quad_perm:[1,0,3,2] row_mask:0xf bank_mask:0xf\n"
    "v_add_f32_dpp v221, v221, v221 quad_perm:[1,0,3,2] row_mask:0xf bank_mask:0xf\n"
    "s_nop 0\n"
    "v_add_f32_dpp v218, v218, v218 quad_perm:[2,3,0,1] row_mask:0xf bank_mask:0xf\n"
    "v_add_f32_dpp v221, v221, v221 quad_perm:[2,3,0,1] row_mask:0xf bank_mask:0xf\n"
    "s_nop 0\n"
    "v_add_f32_dpp v218, v218, v218 row_half_mirror row_mask:0xf bank_mask:0xf\n"
    "v_add_f32_dpp v221, v221, v221 row_half_mirror row_mask:0xf bank_mask:0xf\n"
    "s_nop 0\n"
    "v_add_f32_dpp v218, v218, v218 row_mirror row_mask:0xf bank_mask:0xf\n"
    "v_add_f32_dpp v221, v221, v221 row_mirror row_mask:0xf bank_mask:0xf\n"
    "v_mov_b32 v217, 0x3727c5ac\n"
    "v_fmac_f32 v217, 0x3b000000, v221\n"
    "v_rsq_f32 v221, v217\n"
    "ds_write_b32 v213, v217 offset:256\n"
    "ds_write_b32 v213, v221\n"
    "v_mul_f32 v218, 0xbb000000, v218\n"
    "v_dot2_f32_bf16 v192, v160, s59, v218\n"
    "v_dot2_f32_bf16 v193, v160, s60, v218\n"
    "v_dot2_f32_bf16 v194, v161, s59, v218\n"
    "v_dot2_f32_bf16 v195, v161, s60, v218\n"
    "v_dot2_f32_bf16 v196, v162, s59, v218\n"
    "v_dot2_f32_bf16 v197, v162, s60, v218\n"
    "v_dot2_f32_bf16 v198, v163, s59, v218\n"
    "v_dot2_f32_bf16 v199, v163, s60, v218\n"
    "v_dot2_f32_bf16 v200, v164, s59, v218\n"
    "v_dot2_f32_bf16 v201, v164, s60, v218\n"
    "v_dot2_f32_bf16 v202, v165, s59, v218\n"
    "v_dot2_f32_bf16 v203, v165, s60, v218\n"
    "v_dot2_f32_bf16 v204, v166, s59, v218\n"
    "v_dot2_f32_bf16 v205, v166, s60, v218\n"
    "v_dot2_f32_bf16 v206, v167, s59, v218\n"
    "v_dot2_f32_bf16 v207, v167, s60, v218\n"
    "v_cvt_pk_bf16_f32 v160, v192, v193\n"
    "v_cvt_pk_bf16_f32 v161, v194, v195\n"
    "v_cvt_pk_bf16_f32 v162, v196, v197\n"
    "v_cvt_pk_bf16_f32 v163, v198, v199\n"
    "ds_write_b128 v210, v[160:163]\n"
    "v_dot2c_f32_bf16 v222, v160, v160\n"
    "v_dot2c_f32_bf16 v222, v161, v161\n"
    "v_dot2c_f32_bf16 v222, v162, v162\n"
    "v_dot2c_f32_bf16 v222, v163, v163\n"
    "v_dot2_f32_bf16 v192, v168, s59, v218\n"
    "v_dot2_f32_bf16 v193, v168, s60, v218\n"
    "v_dot2_f32_bf16 v194, v169, s59, v218\n"
    "v_dot2_f32_bf16 v195, v169, s60, v218\n"
    "v_dot2_f32_bf16 v196, v170, s59, v218\n"
    "v_dot2_f32_bf16 v197, v170, s60, v218\n"
    "v_dot2_f32_bf16 v198, v171, s59, v218\n"
    "v_dot2_f32_bf16 v199, v171, s60, v218\n"
    "v_cvt_pk_bf16_f32 v164, v200, v201\n"
    "v_cvt_pk_bf16_f32 v165, v202, v203\n"
    "v_cvt_pk_bf16_f32 v166, v204, v205\n"
    "v_cvt_pk_bf16_f32 v167, v206, v207\n"
    "ds_write_b128 v210, v[164:167] offset:8192\n"
    "v_dot2c_f32_bf16 v222, v164, v164\n"
    "v_dot2c_f32_bf16 v222, v165, v165\n"
    "v_dot2c_f32_bf16 v222, v166, v166\n"
    "v_dot2c_f32_bf16 v222, v167, v167\n"
    "v_dot2_f32_bf16 v200, v172, s59, v218\n"
    "v_dot2_f32_bf16 v201, v172, s60, v218\n"
    "v_dot2_f32_bf16 v202, v173, s59, v218\n"
    "v_dot2_f32_bf16 v203, v173, s60, v218\n"
    "v_dot2_f32_bf16 v204, v174, s59, v218\n"
    "v_dot2_f32_bf16 v205, v174, s60, v218\n"
    "v_dot2_f32_bf16 v206, v175, s59, v218\n"
    "v_dot2_f32_bf16 v207, v175, s60, v218\n"
    "v_cvt_pk_bf16_f32 v168, v192, v193\n"
    "v_cvt_pk_bf16_f32 v169, v194, v195\n"
    "v_cvt_pk_bf16_f32 v170, v196, v197\n"
    "v_cvt_pk_bf16_f32 v171, v198, v199\n"
    "ds_write_b128 v210, v[168:171] offset:16384\n"
    "v_dot2c_f32_bf16 v222, v168, v168\n"
    "v_dot2c_f32_bf16 v222, v169, v169\n"
    "v_dot2c_f32_bf16 v222, v170, v170\n"
    "v_dot2c_f32_bf16 v222, v171, v171\n"
    "v_cvt_pk_bf16_f32 v172, v200, v201\n"
    "v_cvt_pk_bf16_f32 v173, v202, v203\n"
    "v_cvt_pk_bf16_f32 v174, v204, v205\n"
    "v_cvt_pk_bf16_f32 v175, v206, v207\n"
    "ds_write_b128 v210, v[172:175] offset:24576\n"
    "v_dot2c_f32_bf16 v222, v172, v172\n"
    "v_dot2c_f32_bf16 v222, v173, v173\n"
    "v_dot2c_f32_bf16 v222, v174, v174\n"
    "v_dot2c_f32_bf16 v222, v175, v175\n"
    "s_waitcnt vmcnt(3)\n"
    "v_dot2c_f32_bf16 v219, s58, v176\n"
    "v_dot2c_f32_bf16 v219, s58, v177\n"
    "v_dot2c_f32_bf16 v219, s58, v178\n"
    "v_dot2c_f32_bf16 v219, s58, v179\n"
    "s_waitcnt vmcnt(2)\n"
    "v_dot2c_f32_bf16 v219, s58, v180\n"
    "v_dot2c_f32_bf16 v219, s58, v181\n"
    "v_dot2c_f32_bf16 v219, s58, v182\n"
    "v_dot2c_f32_bf16 v219, s58, v183\n"
    "s_waitcnt vmcnt(1)\n"
    "v_dot2c_f32_bf16 v219, s58, v184\n"
    "v_dot2c_f32_bf16 v219, s58, v185\n"
    "v_dot2c_f32_bf16 v219, s58, v186\n"
    "v_dot2c_f32_bf16 v219, s58, v187\n"
    "s_waitcnt vmcnt(0)\n"
    "v_dot2c_f32_bf16 v219, s58, v188\n"
    "v_dot2c_f32_bf16 v219, s58, v189\n"
    "v_dot2c_f32_bf16 v219, s58, v190\n"
    "v_dot2c_f32_bf16 v219, s58, v191\n"
    "s_nop 2\n"
    "v_add_f32_dpp v219, v219, v219 quad_perm:[1,0,3,2] row_mask:0xf bank_mask:0xf\n"
    "v_add_f32_dpp v222, v222, v222 quad_perm:[1,0,3,2] row_mask:0xf bank_mask:0xf\n"
    "s_nop 0\n"
    "v_add_f32_dpp v219, v219, v219 quad_perm:[2,3,0,1] row_mask:0xf bank_mask:0xf\n"
    "v_add_f32_dpp v222, v222, v222 quad_perm:[2,3,0,1] row_mask:0xf bank_mask:0xf\n"
    "s_nop 0\n"
    "v_add_f32_dpp v219, v219, v219 row_half_mirror row_mask:0xf bank_mask:0xf\n"
    "v_add_f32_dpp v222, v222, v222 row_half_mirror row_mask:0xf bank_mask:0xf\n"
    "s_nop 0\n"
    "v_add_f32_dpp v219, v219, v219 row_mirror row_mask:0xf bank_mask:0xf\n"
    "v_add_f32_dpp v222, v222, v222 row_mirror row_mask:0xf bank_mask:0xf\n"
    "v_mov_b32 v218, 0x3727c5ac\n"
    "v_fmac_f32 v218, 0x3b000000, v222\n"
    "v_rsq_f32 v222, v218\n"
    "ds_write_b32 v214, v218 offset:256\n"
    "ds_write_b32 v214, v222\n"
    "v_mul_f32 v219, 0xbb000000, v219\n"
    "v_dot2_f32_bf16 v192, v176, s59, v219\n"
    "v_dot2_f32_bf16 v193, v176, s60, v219\n"
    "v_dot2_f32_bf16 v194, v177, s59, v219\n"
    "v_dot2_f32_bf16 v195, v177, s60, v219\n"
    "v_dot2_f32_bf16 v196, v178, s59, v219\n"
    "v_dot2_f32_bf16 v197, v178, s60, v219\n"
    "v_dot2_f32_bf16 v198, v179, s59, v219\n"
    "v_dot2_f32_bf16 v199, v179, s60, v219\n"
    "v_dot2_f32_bf16 v200, v180, s59, v219\n"
    "v_dot2_f32_bf16 v201, v180, s60, v219\n"
    "v_dot2_f32_bf16 v202, v181, s59, v219\n"
    "v_dot2_f32_bf16 v203, v181, s60, v219\n"
    "v_dot2_f32_bf16 v204, v182, s59, v219\n"
    "v_dot2_f32_bf16 v205, v182, s60, v219\n"
    "v_dot2_f32_bf16 v206, v183, s59, v219\n"
    "v_dot2_f32_bf16 v207, v183, s60, v219\n"
    "v_cvt_pk_bf16_f32 v176, v192, v193\n"
    "v_cvt_pk_bf16_f32 v177, v194, v195\n"
    "v_cvt_pk_bf16_f32 v178, v196, v197\n"
    "v_cvt_pk_bf16_f32 v179, v198, v199\n"
    "ds_write_b128 v211, v[176:179]\n"
    "v_dot2c_f32_bf16 v223, v176, v176\n"
    "v_dot2c_f32_bf16 v223, v177, v177\n"
    "v_dot2c_f32_bf16 v223, v178, v178\n"
    "v_dot2c_f32_bf16 v223, v179, v179\n"
    "v_dot2_f32_bf16 v192, v184, s59, v219\n"
    "v_dot2_f32_bf16 v193, v184, s60, v219\n"
    "v_dot2_f32_bf16 v194, v185, s59, v219\n"
    "v_dot2_f32_bf16 v195, v185, s60, v219\n"
    "v_dot2_f32_bf16 v196, v186, s59, v219\n"
    "v_dot2_f32_bf16 v197, v186, s60, v219\n"
    "v_dot2_f32_bf16 v198, v187, s59, v219\n"
    "v_dot2_f32_bf16 v199, v187, s60, v219\n"
    "v_cvt_pk_bf16_f32 v180, v200, v201\n"
    "v_cvt_pk_bf16_f32 v181, v202, v203\n"
    "v_cvt_pk_bf16_f32 v182, v204, v205\n"
    "v_cvt_pk_bf16_f32 v183, v206, v207\n"
    "ds_write_b128 v211, v[180:183] offset:8192\n"
    "v_dot2c_f32_bf16 v223, v180, v180\n"
    "v_dot2c_f32_bf16 v223, v181, v181\n"
    "v_dot2c_f32_bf16 v223, v182, v182\n"
    "v_dot2c_f32_bf16 v223, v183, v183\n"
    "v_dot2_f32_bf16 v200, v188, s59, v219\n"
    "v_dot2_f32_bf16 v201, v188, s60, v219\n"
    "v_dot2_f32_bf16 v202, v189, s59, v219\n"
    "v_dot2_f32_bf16 v203, v189, s60, v219\n"
    "v_dot2_f32_bf16 v204, v190, s59, v219\n"
    "v_dot2_f32_bf16 v205, v190, s60, v219\n"
    "v_dot2_f32_bf16 v206, v191, s59, v219\n"
    "v_dot2_f32_bf16 v207, v191, s60, v219\n"
    "v_cvt_pk_bf16_f32 v184, v192, v193\n"
    "v_cvt_pk_bf16_f32 v185, v194, v195\n"
    "v_cvt_pk_bf16_f32 v186, v196, v197\n"
    "v_cvt_pk_bf16_f32 v187, v198, v199\n"
    "ds_write_b128 v211, v[184:187] offset:16384\n"
    "v_dot2c_f32_bf16 v223, v184, v184\n"
    "v_dot2c_f32_bf16 v223, v185, v185\n"
    "v_dot2c_f32_bf16 v223, v186, v186\n"
    "v_dot2c_f32_bf16 v223, v187, v187\n"
    "v_cvt_pk_bf16_f32 v188, v200, v201\n"
    "v_cvt_pk_bf16_f32 v189, v202, v203\n"
    "v_cvt_pk_bf16_f32 v190, v204, v205\n"
    "v_cvt_pk_bf16_f32 v191, v206, v207\n"
    "ds_write_b128 v211, v[188:191] offset:24576\n"
    "v_dot2c_f32_bf16 v223, v188, v188\n"
    "v_dot2c_f32_bf16 v223, v189, v189\n"
    "v_dot2c_f32_bf16 v223, v190, v190\n"
    "v_dot2c_f32_bf16 v223, v191, v191\n"
    "s_nop 2\n"
    "v_add_f32_dpp v223, v223, v223 quad_perm:[1,0,3,2] row_mask:0xf bank_mask:0xf\n"
    "s_nop 1\n"
    "v_add_f32_dpp v223, v223, v223 quad_perm:[2,3,0,1] row_mask:0xf bank_mask:0xf\n"
    "s_nop 1\n"
    "v_add_f32_dpp v223, v223, v223 row_half_mirror row_mask:0xf bank_mask:0xf\n"
    "s_nop 1\n"
    "v_add_f32_dpp v223, v223, v223 row_mirror row_mask:0xf bank_mask:0xf\n"
    "v_mov_b32 v219, 0x3727c5ac\n"
    "v_fmac_f32 v219, 0x3b000000, v223\n"
    "v_rsq_f32 v223, v219\n"
    "ds_write_b32 v215, v219 offset:256\n"
    "ds_write_b32 v215, v223\n"
    "s_waitcnt lgkmcnt(0)\n"
    "s_barrier\n"
    "s_setprio 0\n"
    "ds_read_b64 v[248:249], v247 offset:48\n"
    "s_lshl_b32 s69, s54, 15\n"
    "s_add_u32 s69, s69, 262144\n"
    "s_mov_b32 s68, s69\n"
    "s_mov_b64 s[32:33], s[28:29]\n"
    "s_add_u32 s34, s69, 32768\n"
    "s_mov_b32 s35, 0x20000\n"
    "s_cmp_eq_u32 s54, 0\n"
    "s_cselect_b64 s[36:37], s[14:15], s[16:17]\n"
    "s_mul_i32 s60, s79, 2048\n"
    "s_mov_b32 s38, 1835008\n"
    "s_mov_b32 s73, 524288\n"
    "v_and_b32 v5, 15, v243\n"
    "v_lshlrev_b32 v5, 7, v5\n"
    "s_cmp_lt_u32 s54, 2\n"
    "s_cbranch_scc1 .La_w01\n"
    "s_cmp_eq_u32 s54, 2\n"
    "s_cselect_b64 s[36:37], s[18:19], s[12:13]\n"
    "s_cselect_b32 s38, s38, 524288\n"
    "s_cselect_b32 s61, 0, 2097152\n"
    "s_sub_u32 s60, s60, s61\n"
    "s_sub_u32 s60, s60, 2359296\n"
    ".La_w01:\n"
    "s_add_u32 s60, s60, 262144\n"
    "s_and_b32 s37, s37, 0xffff\n"
    "s_mov_b32 s39, 0x20000\n"
    "v_and_b32 v1, 27, v243\n"
    "v_cmp_eq_u32 vcc, 16, v1\n"
    "v_mov_b32 v1, 0x3f803f80\n"
    "s_nop 0\n"
    "v_cndmask_b32 v6, 0, v1, vcc\n"
    "v_cndmask_b32 v7, 0, v1, vcc\n"
    "v_mov_b32 v196, v6\n"
    "v_mov_b32 v197, v6\n"
    "v_mov_b32 v198, v6\n"
    "v_mov_b32 v199, v6\n"
    "v_mov_b32 v200, v6\n"
    "v_mov_b32 v201, v6\n"
    "v_mov_b32 v202, v6\n"
    "v_mov_b32 v203, v6\n"
    "v_mov_b32 v208, v6\n"
    "v_mov_b32 v209, v6\n"
    "v_mov_b32 v210, v6\n"
    "v_mov_b32 v211, v6\n"
    "v_mov_b32 v212, v6\n"
    "v_mov_b32 v213, v6\n"
    "v_mov_b32 v214, v6\n"
    "v_mov_b32 v215, v6\n"
    "v_mov_b32 v220, v6\n"
    "v_mov_b32 v221, v6\n"
    "v_mov_b32 v222, v6\n"
    "v_mov_b32 v223, v6\n"
    "v_mov_b32 v224, v6\n"
    "v_mov_b32 v225, v6\n"
    "v_mov_b32 v226, v6\n"
    "v_mov_b32 v227, v6\n"
    "v_mov_b32 v232, v6\n"
    "v_mov_b32 v233, v6\n"
    "v_mov_b32 v234, v6\n"
    "v_mov_b32 v235, v6\n"
    "v_mov_b32 v236, v6\n"
    "v_mov_b32 v237, v6\n"
    "v_mov_b32 v238, v6\n"
    "v_mov_b32 v239, v6\n"
    "s_waitcnt lgkmcnt(0)\n"
    "v_readfirstlane_b32 s56, v248\n"
    "v_readfirstlane_b32 s57, v249\n"
    "v_add_u32 v248, s60, v5\n"
    "s_mov_b32 s71, 2097152\n"
    "buffer_load_dwordx2 v[2:3], v0, s[28:31], s74 offen sc1\n"
    "s_branch .Lkv_chk\n"
    ".Lkv_poll:\n"
    "buffer_load_dwordx2 v[2:3], v0, s[28:31], s74 offen sc1\n"
    ".Lkv_chk:\n"
    "s_waitcnt vmcnt(0)\n"
    "v_and_b32 v4, 1, v2\n"
    "v_or_b32 v2, 1, v2\n"
    "v_cmp_ne_u64 vcc, s[56:57], v[2:3]\n"
    "s_cbranch_vccz .Lkv_ok\n"
    "s_sleep 4\n"
    "s_sub_u32 s71, s71, 1\n"
    "s_cbranch_scc0 .Lkv_poll\n"
    ".Lkv_ok:\n"
    "buffer_load_dwordx4 v[192:195], v242, s[32:35], s68 offen sc1\n"
    "s_mov_b64 exec, s[76:77]\n"
    "buffer_load_dwordx4 v[196:199], v241, s[32:35], s68 offen offset:1024 sc1\n"
    "buffer_load_dwordx4 v[200:203], v241, s[32:35], s68 offen offset:1536 sc1\n"
    "s_mov_b64 exec, -1\n"
    "buffer_load_dwordx4 v[252:255], v244, s[24:27], 0 offen\n"
    "buffer_load_dwordx4 v[204:207], v242, s[32:35], s68 offen offset:2048 sc1\n"
    "s_mov_b64 exec, s[76:77]\n"
    "buffer_load_dwordx4 v[208:211], v241, s[32:35], s68 offen offset:3072 sc1\n"
    "buffer_load_dwordx4 v[212:215], v241, s[32:35], s68 offen offset:3584 sc1\n"
    "s_mov_b64 exec, -1\n"
    "s_add_u32 s68, s68, 4096\n"
    "buffer_load_dwordx4 v[216:219], v242, s[32:35], s68 offen sc1\n"
    "s_mov_b64 exec, s[76:77]\n"
    "buffer_load_dwordx4 v[220:223], v241, s[32:35], s68 offen offset:1024 sc1\n"
    "buffer_load_dwordx4 v[224:227], v241, s[32:35], s68 offen offset:1536 sc1\n"
    "s_mov_b64 exec, -1\n"
    "s_mov_b64 exec, 1\n"
    "ds_add_u32 v247, v250 offset:36\n"
    "s_mov_b64 exec, -1\n"
    "v_cmp_eq_u32 vcc, 0, v4\n"
    "s_nop 0\n"
    "s_cmp_lg_u64 vcc, 0\n"
    "s_cselect_b32 s62, 0x7f800000, 0\n"
    "s_mov_b32 s78, 0\n"
    "v_mov_b32 v0, 0\n"
    "v_mov_b32 v1, 0\n"
    "v_mov_b32 v2, 0\n"
    "v_mov_b32 v3, 0\n"
    "v_mov_b32 v4, 0\n"
    "v_mov_b32 v5, 0\n"
    "v_mov_b32 v6, 0\n"
    "v_mov_b32 v7, 0\n"
    "v_mov_b32 v184, 0\n"
    "v_mov_b32 v185, 0\n"
    "v_mov_b32 v186, 0\n"
    "v_mov_b32 v187, 0\n"
    "v_mov_b32 v188, 0\n"
    "v_mov_b32 v189, 0\n"
    "v_mov_b32 v190, 0\n"
    "v_mov_b32 v191, 0\n"
    "v_mov_b32 v8, s62\n"
    "v_mov_b32 v24, s62\n"
    "v_mov_b32 v40, s62\n"
    "v_mov_b32 v56, s62\n"
    "v_mov_b32 v72, s62\n"
    "v_mov_b32 v88, s62\n"
    "v_mov_b32 v104, s62\n"
    "v_mov_b32 v120, s62\n"
    "s_lshl_b32 s58, s52, 3\n"
    "s_add_u32 s58, s58, s53\n"
    "s_lshl_b32 s58, s58, 2\n"
    "s_add_u32 s58, s58, s54\n"
    "s_lshl_b32 s72, s58, 11\n"
    "s_add_u32 s72, s72, 1048576\n"
    "s_add_u32 s96, s54, 3\n"
    "s_and_b32 s96, s96, 3\n"
    "s_mul_i32 s96, s96, 13824\n"
    "s_add_u32 s97, s54, 2\n"
    "s_and_b32 s97, s97, 3\n"
    "s_mul_i32 s97, s97, 13824\n"
    "s_add_u32 s98, s54, 1\n"
    "s_and_b32 s98, s98, 3\n"
    "s_mul_i32 s98, s98, 13824\n"
    "s_add_u32 s99, s54, 0\n"
    "s_and_b32 s99, s99, 3\n"
    "s_mul_i32 s99, s99, 13824\n"
    "s_add_u32 s58, s54, 1\n"
    "s_and_b32 s58, s58, 3\n"
    "s_lshl_b32 s58, s58, 11\n"
    "s_add_u32 s58, s58, 147456\n"
    "v_add_u32 v160, s58, v242\n"
    "s_add_u32 s59, s54, 2\n"
    "s_and_b32 s59, s59, 3\n"
    "s_lshl_b32 s59, s59, 11\n"
    "s_add_u32 s59, s59, 147456\n"
    "v_add_u32 v161, s59, v242\n"
    "s_add_u32 s60, s54, 3\n"
    "s_and_b32 s60, s60, 3\n"
    "s_lshl_b32 s60, s60, 11\n"
    "s_add_u32 s60, s60, 147456\n"
    "v_add_u32 v162, s60, v242\n"
    "s_add_u32 s61, s54, 4\n"
    "s_and_b32 s61, s61, 3\n"
    "s_lshl_b32 s61, s61, 11\n"
    "s_add_u32 s61, s61, 147456\n"
    "v_add_u32 v163, s61, v242\n"
    "v_and_b32 v164, 31, v243\n"
    "v_lshlrev_b32 v164, 2, v164\n"
    "v_add_u32 v164, 162048, v164\n"
    "s_mov_b32 s71, 8388608\n"
    ".Lq819_spin:\n"
    "ds_read_b32 v165, v247\n"
    "s_waitcnt lgkmcnt(0)\n"
    "v_cmp_gt_u32 vcc, 4, v165\n"
    "s_cbranch_vccz .Lq819_go\n"
    "s_sleep 1\n"
    "s_sub_u32 s71, s71, 1\n"
    "s_cbranch_scc0 .Lq819_spin\n"
    ".Lq819_go:\n"
    "ds_read_b128 v[128:131], v160\n"
    "ds_read_b128 v[132:135], v160 offset:1024\n"
    "ds_read_b128 v[136:139], v161\n"
    "ds_read_b128 v[140:143], v161 offset:1024\n"
    "ds_read_b128 v[144:147], v162\n"
    "ds_read_b128 v[148:151], v162 offset:1024\n"
    "ds_read_b128 v[152:155], v163\n"
    "ds_read_b128 v[156:159], v163 offset:1024\n"
    "ds_read_b32 v245, v164\n"
    "ds_read_b32 v246, v164 offset:128\n"
    "s_waitcnt vmcnt(7) lgkmcnt(0)\n"
    "s_mov_b64 exec, 1\n"
    "ds_add_u32 v247, v250 offset:20\n"
    "s_mov_b64 exec, -1\n"
    "v_mfma_f32_32x32x16_bf16 v[160:175], v[192:195], v[128:131], 0\n"
    "s_mov_b32 s71, 3\n"
    "s_nop 10\n"
    ".p2align 6\n"
    ".La_loop:\n"
    "v_exp_f32 v160, v160\n"
    "v_exp_f32 v161, v161\n"
    "v_smfmac_f32_32x32x32_bf16 v[112:127], v[236:239], v[184:191], v240\n"
    "v_exp_f32 v162, v162\n"
    "v_exp_f32 v163, v163\n"
    "v_exp_f32 v164, v164\n"
    "v_mfma_f32_32x32x16_bf16 v[176:191], v[192:195], v[132:135], 0\n"
    "buffer_load_dwordx4 v[228:231], v242, s[32:35], s68 offen offset:2048 sc1\n"
    "s_mov_b64 exec, s[76:77]\n"
    "buffer_load_dwordx4 v[232:235], v241, s[32:35], s68 offen offset:3072 sc1\n"
    "buffer_load_dwordx4 v[236:239], v241, s[32:35], s68 offen offset:3584 sc1\n"
    "buffer_load_dword v251, v248, s[36:39], 0 offen\n"
    "v_add_u32 v248, s73, v248\n"
    "s_mov_b64 exec, -1\n"
    "s_add_u32 s68, s68, 4096\n"
    "v_exp_f32 v165, v165\n"
    "v_exp_f32 v166, v166\n"
    "v_exp_f32 v167, v167\n"
    "v_exp_f32 v168, v168\n"
    "v_exp_f32 v169, v169\n"
    "v_exp_f32 v170, v170\n"
    "v_smfmac_f32_32x32x32_bf16 v[0:15], v[196:199], v[160:167], v240\n"
    "v_exp_f32 v171, v171\n"
    "v_exp_f32 v172, v172\n"
    "v_exp_f32 v173, v173\n"
    "v_exp_f32 v174, v174\n"
    "v_exp_f32 v175, v175\n"
    "v_exp_f32 v176, v176\n"
    "v_exp_f32 v177, v177\n"
    "v_smfmac_f32_32x32x32_bf16 v[0:15], v[200:203], v[168:175], v240\n"
    "v_exp_f32 v178, v178\n"
    "v_exp_f32 v179, v179\n"
    "v_exp_f32 v180, v180\n"
    "v_mfma_f32_32x32x16_bf16 v[160:175], v[192:195], v[136:139], 0\n"
    "v_exp_f32 v181, v181\n"
    "v_exp_f32 v182, v182\n"
    "v_exp_f32 v183, v183\n"
    "v_exp_f32 v184, v184\n"
    "v_exp_f32 v185, v185\n"
    "v_exp_f32 v186, v186\n"
    "v_smfmac_f32_32x32x32_bf16 v[16:31], v[196:199], v[176:183], v240\n"
    "v_exp_f32 v187, v187\n"
    "v_exp_f32 v188, v188\n"
    "v_exp_f32 v189, v189\n"
    "v_exp_f32 v190, v190\n"
    "v_exp_f32 v191, v191\n"
    "v_exp_f32 v160, v160\n"
    "v_exp_f32 v161, v161\n"
    "v_smfmac_f32_32x32x32_bf16 v[16:31], v[200:203], v[184:191], v240\n"
    "v_exp_f32 v162, v162\n"
    "v_exp_f32 v163, v163\n"
    "v_exp_f32 v164, v164\n"
    "v_mfma_f32_32x32x16_bf16 v[176:191], v[192:195], v[140:143], 0\n"
    "v_exp_f32 v165, v165\n"
    "v_exp_f32 v166, v166\n"
    "v_exp_f32 v167, v167\n"
    "v_exp_f32 v168, v168\n"
    "v_exp_f32 v169, v169\n"
    "v_exp_f32 v170, v170\n"
    "v_smfmac_f32_32x32x32_bf16 v[32:47], v[196:199], v[160:167], v240\n"
    "v_exp_f32 v171, v171\n"
    "v_exp_f32 v172, v172\n"
    "v_exp_f32 v173, v173\n"
    "v_exp_f32 v174, v174\n"
    "v_exp_f32 v175, v175\n"
    "v_exp_f32 v176, v176\n"
    "v_exp_f32 v177, v177\n"
    "v_smfmac_f32_32x32x32_bf16 v[32:47], v[200:203], v[168:175], v240\n"
    "v_exp_f32 v178, v178\n"
    "v_exp_f32 v179, v179\n"
    "v_exp_f32 v180, v180\n"
    "v_mfma_f32_32x32x16_bf16 v[160:175], v[192:195], v[144:147], 0\n"
    "v_exp_f32 v181, v181\n"
    "v_exp_f32 v182, v182\n"
    "v_exp_f32 v183, v183\n"
    "v_exp_f32 v184, v184\n"
    "v_exp_f32 v185, v185\n"
    "v_exp_f32 v186, v186\n"
    "v_smfmac_f32_32x32x32_bf16 v[48:63], v[196:199], v[176:183], v240\n"
    "v_exp_f32 v187, v187\n"
    "v_exp_f32 v188, v188\n"
    "v_exp_f32 v189, v189\n"
    "v_exp_f32 v190, v190\n"
    "v_exp_f32 v191, v191\n"
    "v_exp_f32 v160, v160\n"
    "v_exp_f32 v161, v161\n"
    "v_smfmac_f32_32x32x32_bf16 v[48:63], v[200:203], v[184:191], v240\n"
    "v_exp_f32 v162, v162\n"
    "v_exp_f32 v163, v163\n"
    "v_exp_f32 v164, v164\n"
    "v_mfma_f32_32x32x16_bf16 v[176:191], v[192:195], v[148:151], 0\n"
    "v_exp_f32 v165, v165\n"
    "v_exp_f32 v166, v166\n"
    "v_exp_f32 v167, v167\n"
    "v_exp_f32 v168, v168\n"
    "v_exp_f32 v169, v169\n"
    "v_exp_f32 v170, v170\n"
    "v_smfmac_f32_32x32x32_bf16 v[64:79], v[196:199], v[160:167], v240\n"
    "v_exp_f32 v171, v171\n"
    "v_exp_f32 v172, v172\n"
    "v_exp_f32 v173, v173\n"
    "v_exp_f32 v174, v174\n"
    "v_exp_f32 v175, v175\n"
    "v_exp_f32 v176, v176\n"
    "v_exp_f32 v177, v177\n"
    "v_smfmac_f32_32x32x32_bf16 v[64:79], v[200:203], v[168:175], v240\n"
    "v_exp_f32 v178, v178\n"
    "v_exp_f32 v179, v179\n"
    "v_exp_f32 v180, v180\n"
    "v_mfma_f32_32x32x16_bf16 v[160:175], v[192:195], v[152:155], 0\n"
    "v_exp_f32 v181, v181\n"
    "v_exp_f32 v182, v182\n"
    "v_exp_f32 v183, v183\n"
    "v_exp_f32 v184, v184\n"
    "v_exp_f32 v185, v185\n"
    "v_exp_f32 v186, v186\n"
    "v_smfmac_f32_32x32x32_bf16 v[80:95], v[196:199], v[176:183], v240\n"
    "v_exp_f32 v187, v187\n"
    "v_exp_f32 v188, v188\n"
    "v_exp_f32 v189, v189\n"
    "v_exp_f32 v190, v190\n"
    "v_exp_f32 v191, v191\n"
    "v_exp_f32 v160, v160\n"
    "v_exp_f32 v161, v161\n"
    "v_smfmac_f32_32x32x32_bf16 v[80:95], v[200:203], v[184:191], v240\n"
    "v_exp_f32 v162, v162\n"
    "v_exp_f32 v163, v163\n"
    "v_exp_f32 v164, v164\n"
    "v_mfma_f32_32x32x16_bf16 v[176:191], v[192:195], v[156:159], 0\n"
    "v_exp_f32 v165, v165\n"
    "v_exp_f32 v166, v166\n"
    "v_exp_f32 v167, v167\n"
    "v_exp_f32 v168, v168\n"
    "v_exp_f32 v169, v169\n"
    "v_exp_f32 v170, v170\n"
    "v_smfmac_f32_32x32x32_bf16 v[96:111], v[196:199], v[160:167], v240\n"
    "v_exp_f32 v171, v171\n"
    "v_exp_f32 v172, v172\n"
    "v_exp_f32 v173, v173\n"
    "v_exp_f32 v174, v174\n"
    "v_exp_f32 v175, v175\n"
    "v_exp_f32 v176, v176\n"
    "v_exp_f32 v177, v177\n"
    "v_smfmac_f32_32x32x32_bf16 v[96:111], v[200:203], v[168:175], v240\n"
    "v_exp_f32 v178, v178\n"
    "v_exp_f32 v179, v179\n"
    "v_exp_f32 v180, v180\n"
    "s_waitcnt vmcnt(7)\n"
    "v_mfma_f32_32x32x16_bf16 v[160:175], v[204:207], v[128:131], 0\n"
    "s_sub_u32 s71, s71, 1\n"
    "v_exp_f32 v181, v181\n"
    "v_exp_f32 v182, v182\n"
    "v_exp_f32 v183, v183\n"
    "v_exp_f32 v184, v184\n"
    "v_exp_f32 v185, v185\n"
    "v_exp_f32 v186, v186\n"
    "v_smfmac_f32_32x32x32_bf16 v[112:127], v[196:199], v[176:183], v240\n"
    "v_exp_f32 v187, v187\n"
    "v_exp_f32 v188, v188\n"
    "v_exp_f32 v189, v189\n"
    "v_exp_f32 v190, v190\n"
    "v_exp_f32 v191, v191\n"
    "s_cbranch_scc1 .La_last\n"
    "v_exp_f32 v160, v160\n"
    "v_exp_f32 v161, v161\n"
    "v_smfmac_f32_32x32x32_bf16 v[112:127], v[200:203], v[184:191], v240\n"
    "v_exp_f32 v162, v162\n"
    "v_exp_f32 v163, v163\n"
    "v_exp_f32 v164, v164\n"
    "v_mfma_f32_32x32x16_bf16 v[176:191], v[204:207], v[132:135], 0\n"
    "buffer_load_dwordx4 v[192:195], v242, s[32:35], s68 offen sc1\n"
    "s_mov_b64 exec, s[76:77]\n"
    "buffer_load_dwordx4 v[196:199], v241, s[32:35], s68 offen offset:1024 sc1\n"
    "buffer_load_dwordx4 v[200:203], v241, s[32:35], s68 offen offset:1536 sc1\n"
    "buffer_load_dword v251, v248, s[36:39], 0 offen\n"
    "v_add_u32 v248, s73, v248\n"
    "s_mov_b64 exec, -1\n"
    "v_exp_f32 v165, v165\n"
    "v_exp_f32 v166, v166\n"
    "v_exp_f32 v167, v167\n"
    "v_exp_f32 v168, v168\n"
    "v_exp_f32 v169, v169\n"
    "v_exp_f32 v170, v170\n"
    "v_smfmac_f32_32x32x32_bf16 v[0:15], v[208:211], v[160:167], v240\n"
    "v_exp_f32 v171, v171\n"
    "v_exp_f32 v172, v172\n"
    "v_exp_f32 v173, v173\n"
    "v_exp_f32 v174, v174\n"
    "v_exp_f32 v175, v175\n"
    "v_exp_f32 v176, v176\n"
    "v_exp_f32 v177, v177\n"
    "v_smfmac_f32_32x32x32_bf16 v[0:15], v[212:215], v[168:175], v240\n"
    "v_exp_f32 v178, v178\n"
    "v_exp_f32 v179, v179\n"
    "v_exp_f32 v180, v180\n"
    "v_mfma_f32_32x32x16_bf16 v[160:175], v[204:207], v[136:139], 0\n"
    "v_exp_f32 v181, v181\n"
    "v_exp_f32 v182, v182\n"
    "v_exp_f32 v183, v183\n"
    "v_exp_f32 v184, v184\n"
    "v_exp_f32 v185, v185\n"
    "v_exp_f32 v186, v186\n"
    "v_smfmac_f32_32x32x32_bf16 v[16:31], v[208:211], v[176:183], v240\n"
    "v_exp_f32 v187, v187\n"
    "v_exp_f32 v188, v188\n"
    "v_exp_f32 v189, v189\n"
    "v_exp_f32 v190, v190\n"
    "v_exp_f32 v191, v191\n"
    "v_exp_f32 v160, v160\n"
    "v_exp_f32 v161, v161\n"
    "v_smfmac_f32_32x32x32_bf16 v[16:31], v[212:215], v[184:191], v240\n"
    "v_exp_f32 v162, v162\n"
    "v_exp_f32 v163, v163\n"
    "v_exp_f32 v164, v164\n"
    "v_mfma_f32_32x32x16_bf16 v[176:191], v[204:207], v[140:143], 0\n"
    "v_exp_f32 v165, v165\n"
    "v_exp_f32 v166, v166\n"
    "v_exp_f32 v167, v167\n"
    "v_exp_f32 v168, v168\n"
    "v_exp_f32 v169, v169\n"
    "v_exp_f32 v170, v170\n"
    "v_smfmac_f32_32x32x32_bf16 v[32:47], v[208:211], v[160:167], v240\n"
    "v_exp_f32 v171, v171\n"
    "v_exp_f32 v172, v172\n"
    "v_exp_f32 v173, v173\n"
    "v_exp_f32 v174, v174\n"
    "v_exp_f32 v175, v175\n"
    "v_exp_f32 v176, v176\n"
    "v_exp_f32 v177, v177\n"
    "v_smfmac_f32_32x32x32_bf16 v[32:47], v[212:215], v[168:175], v240\n"
    "v_exp_f32 v178, v178\n"
    "v_exp_f32 v179, v179\n"
    "v_exp_f32 v180, v180\n"
    "v_mfma_f32_32x32x16_bf16 v[160:175], v[204:207], v[144:147], 0\n"
    "v_exp_f32 v181, v181\n"
    "v_exp_f32 v182, v182\n"
    "v_exp_f32 v183, v183\n"
    "v_exp_f32 v184, v184\n"
    "v_exp_f32 v185, v185\n"
    "v_exp_f32 v186, v186\n"
    "v_smfmac_f32_32x32x32_bf16 v[48:63], v[208:211], v[176:183], v240\n"
    "v_exp_f32 v187, v187\n"
    "v_exp_f32 v188, v188\n"
    "v_exp_f32 v189, v189\n"
    "v_exp_f32 v190, v190\n"
    "v_exp_f32 v191, v191\n"
    "v_exp_f32 v160, v160\n"
    "v_exp_f32 v161, v161\n"
    "v_smfmac_f32_32x32x32_bf16 v[48:63], v[212:215], v[184:191], v240\n"
    "v_exp_f32 v162, v162\n"
    "v_exp_f32 v163, v163\n"
    "v_exp_f32 v164, v164\n"
    "v_mfma_f32_32x32x16_bf16 v[176:191], v[204:207], v[148:151], 0\n"
    "v_exp_f32 v165, v165\n"
    "v_exp_f32 v166, v166\n"
    "v_exp_f32 v167, v167\n"
    "v_exp_f32 v168, v168\n"
    "v_exp_f32 v169, v169\n"
    "v_exp_f32 v170, v170\n"
    "v_smfmac_f32_32x32x32_bf16 v[64:79], v[208:211], v[160:167], v240\n"
    "v_exp_f32 v171, v171\n"
    "v_exp_f32 v172, v172\n"
    "v_exp_f32 v173, v173\n"
    "v_exp_f32 v174, v174\n"
    "v_exp_f32 v175, v175\n"
    "v_exp_f32 v176, v176\n"
    "v_exp_f32 v177, v177\n"
    "v_smfmac_f32_32x32x32_bf16 v[64:79], v[212:215], v[168:175], v240\n"
    "v_exp_f32 v178, v178\n"
    "v_exp_f32 v179, v179\n"
    "v_exp_f32 v180, v180\n"
    "v_mfma_f32_32x32x16_bf16 v[160:175], v[204:207], v[152:155], 0\n"
    "v_exp_f32 v181, v181\n"
    "v_exp_f32 v182, v182\n"
    "v_exp_f32 v183, v183\n"
    "v_exp_f32 v184, v184\n"
    "v_exp_f32 v185, v185\n"
    "v_exp_f32 v186, v186\n"
    "v_smfmac_f32_32x32x32_bf16 v[80:95], v[208:211], v[176:183], v240\n"
    "v_exp_f32 v187, v187\n"
    "v_exp_f32 v188, v188\n"
    "v_exp_f32 v189, v189\n"
    "v_exp_f32 v190, v190\n"
    "v_exp_f32 v191, v191\n"
    "v_exp_f32 v160, v160\n"
    "v_exp_f32 v161, v161\n"
    "v_smfmac_f32_32x32x32_bf16 v[80:95], v[212:215], v[184:191], v240\n"
    "v_exp_f32 v162, v162\n"
    "v_exp_f32 v163, v163\n"
    "v_exp_f32 v164, v164\n"
    "v_mfma_f32_32x32x16_bf16 v[176:191], v[204:207], v[156:159], 0\n"
    "v_exp_f32 v165, v165\n"
    "v_exp_f32 v166, v166\n"
    "v_exp_f32 v167, v167\n"
    "v_exp_f32 v168, v168\n"
    "v_exp_f32 v169, v169\n"
    "v_exp_f32 v170, v170\n"
    "v_smfmac_f32_32x32x32_bf16 v[96:111], v[208:211], v[160:167], v240\n"
    "v_exp_f32 v171, v171\n"
    "v_exp_f32 v172, v172\n"
    "v_exp_f32 v173, v173\n"
    "v_exp_f32 v174, v174\n"
    "v_exp_f32 v175, v175\n"
    "v_exp_f32 v176, v176\n"
    "v_exp_f32 v177, v177\n"
    "v_smfmac_f32_32x32x32_bf16 v[96:111], v[212:215], v[168:175], v240\n"
    "v_exp_f32 v178, v178\n"
    "v_exp_f32 v179, v179\n"
    "v_exp_f32 v180, v180\n"
    "s_waitcnt vmcnt(8)\n"
    "v_mfma_f32_32x32x16_bf16 v[160:175], v[216:219], v[128:131], 0\n"
    "v_exp_f32 v181, v181\n"
    "v_exp_f32 v182, v182\n"
    "v_exp_f32 v183, v183\n"
    "v_exp_f32 v184, v184\n"
    "v_exp_f32 v185, v185\n"
    "v_exp_f32 v186, v186\n"
    "v_smfmac_f32_32x32x32_bf16 v[112:127], v[208:211], v[176:183], v240\n"
    "v_exp_f32 v187, v187\n"
    "v_exp_f32 v188, v188\n"
    "v_exp_f32 v189, v189\n"
    "v_exp_f32 v190, v190\n"
    "v_exp_f32 v191, v191\n"
    "v_exp_f32 v160, v160\n"
    "v_exp_f32 v161, v161\n"
    "v_smfmac_f32_32x32x32_bf16 v[112:127], v[212:215], v[184:191], v240\n"
    "v_exp_f32 v162, v162\n"
    "v_exp_f32 v163, v163\n"
    "v_exp_f32 v164, v164\n"
    "v_mfma_f32_32x32x16_bf16 v[176:191], v[216:219], v[132:135], 0\n"
    "buffer_load_dwordx4 v[204:207], v242, s[32:35], s68 offen offset:2048 sc1\n"
    "s_mov_b64 exec, s[76:77]\n"
    "buffer_load_dwordx4 v[208:211], v241, s[32:35], s68 offen offset:3072 sc1\n"
    "buffer_load_dwordx4 v[212:215], v241, s[32:35], s68 offen offset:3584 sc1\n"
    "buffer_load_dword v251, v248, s[36:39], 0 offen\n"
    "v_add_u32 v248, s73, v248\n"
    "s_mov_b64 exec, -1\n"
    "s_add_u32 s68, s68, 4096\n"
    "v_exp_f32 v165, v165\n"
    "v_exp_f32 v166, v166\n"
    "v_exp_f32 v167, v167\n"
    "v_exp_f32 v168, v168\n"
    "v_exp_f32 v169, v169\n"
    "v_exp_f32 v170, v170\n"
    "v_smfmac_f32_32x32x32_bf16 v[0:15], v[220:223], v[160:167], v240\n"
    "v_exp_f32 v171, v171\n"
    "v_exp_f32 v172, v172\n"
    "v_exp_f32 v173, v173\n"
    "v_exp_f32 v174, v174\n"
    "v_exp_f32 v175, v175\n"
    "v_exp_f32 v176, v176\n"
    "v_exp_f32 v177, v177\n"
    "v_smfmac_f32_32x32x32_bf16 v[0:15], v[224:227], v[168:175], v240\n"
    "v_exp_f32 v178, v178\n"
    "v_exp_f32 v179, v179\n"
    "v_exp_f32 v180, v180\n"
    "v_mfma_f32_32x32x16_bf16 v[160:175], v[216:219], v[136:139], 0\n"
    "v_exp_f32 v181, v181\n"
    "v_exp_f32 v182, v182\n"
    "v_exp_f32 v183, v183\n"
    "v_exp_f32 v184, v184\n"
    "v_exp_f32 v185, v185\n"
    "v_exp_f32 v186, v186\n"
    "v_smfmac_f32_32x32x32_bf16 v[16:31], v[220:223], v[176:183], v240\n"
    "v_exp_f32 v187, v187\n"
    "v_exp_f32 v188, v188\n"
    "v_exp_f32 v189, v189\n"
    "v_exp_f32 v190, v190\n"
    "v_exp_f32 v191, v191\n"
    "v_exp_f32 v160, v160\n"
    "v_exp_f32 v161, v161\n"
    "v_smfmac_f32_32x32x32_bf16 v[16:31], v[224:227], v[184:191], v240\n"
    "v_exp_f32 v162, v162\n"
    "v_exp_f32 v163, v163\n"
    "v_exp_f32 v164, v164\n"
    "v_mfma_f32_32x32x16_bf16 v[176:191], v[216:219], v[140:143], 0\n"
    "v_exp_f32 v165, v165\n"
    "v_exp_f32 v166, v166\n"
    "v_exp_f32 v167, v167\n"
    "v_exp_f32 v168, v168\n"
    "v_exp_f32 v169, v169\n"
    "v_exp_f32 v170, v170\n"
    "v_smfmac_f32_32x32x32_bf16 v[32:47], v[220:223], v[160:167], v240\n"
    "v_exp_f32 v171, v171\n"
    "v_exp_f32 v172, v172\n"
    "v_exp_f32 v173, v173\n"
    "v_exp_f32 v174, v174\n"
    "v_exp_f32 v175, v175\n"
    "v_exp_f32 v176, v176\n"
    "v_exp_f32 v177, v177\n"
    "v_smfmac_f32_32x32x32_bf16 v[32:47], v[224:227], v[168:175], v240\n"
    "v_exp_f32 v178, v178\n"
    "v_exp_f32 v179, v179\n"
    "v_exp_f32 v180, v180\n"
    "v_mfma_f32_32x32x16_bf16 v[160:175], v[216:219], v[144:147], 0\n"
    "v_exp_f32 v181, v181\n"
    "v_exp_f32 v182, v182\n"
    "v_exp_f32 v183, v183\n"
    "v_exp_f32 v184, v184\n"
    "v_exp_f32 v185, v185\n"
    "v_exp_f32 v186, v186\n"
    "v_smfmac_f32_32x32x32_bf16 v[48:63], v[220:223], v[176:183], v240\n"
    "v_exp_f32 v187, v187\n"
    "v_exp_f32 v188, v188\n"
    "v_exp_f32 v189, v189\n"
    "v_exp_f32 v190, v190\n"
    "v_exp_f32 v191, v191\n"
    "v_exp_f32 v160, v160\n"
    "v_exp_f32 v161, v161\n"
    "v_smfmac_f32_32x32x32_bf16 v[48:63], v[224:227], v[184:191], v240\n"
    "v_exp_f32 v162, v162\n"
    "v_exp_f32 v163, v163\n"
    "v_exp_f32 v164, v164\n"
    "v_mfma_f32_32x32x16_bf16 v[176:191], v[216:219], v[148:151], 0\n"
    "v_exp_f32 v165, v165\n"
    "v_exp_f32 v166, v166\n"
    "v_exp_f32 v167, v167\n"
    "v_exp_f32 v168, v168\n"
    "v_exp_f32 v169, v169\n"
    "v_exp_f32 v170, v170\n"
    "v_smfmac_f32_32x32x32_bf16 v[64:79], v[220:223], v[160:167], v240\n"
    "v_exp_f32 v171, v171\n"
    "v_exp_f32 v172, v172\n"
    "v_exp_f32 v173, v173\n"
    "v_exp_f32 v174, v174\n"
    "v_exp_f32 v175, v175\n"
    "v_exp_f32 v176, v176\n"
    "v_exp_f32 v177, v177\n"
    "v_smfmac_f32_32x32x32_bf16 v[64:79], v[224:227], v[168:175], v240\n"
    "v_exp_f32 v178, v178\n"
    "v_exp_f32 v179, v179\n"
    "v_exp_f32 v180, v180\n"
    "v_mfma_f32_32x32x16_bf16 v[160:175], v[216:219], v[152:155], 0\n"
    "v_exp_f32 v181, v181\n"
    "v_exp_f32 v182, v182\n"
    "v_exp_f32 v183, v183\n"
    "v_exp_f32 v184, v184\n"
    "v_exp_f32 v185, v185\n"
    "v_exp_f32 v186, v186\n"
    "v_smfmac_f32_32x32x32_bf16 v[80:95], v[220:223], v[176:183], v240\n"
    "v_exp_f32 v187, v187\n"
    "v_exp_f32 v188, v188\n"
    "v_exp_f32 v189, v189\n"
    "v_exp_f32 v190, v190\n"
    "v_exp_f32 v191, v191\n"
    "v_exp_f32 v160, v160\n"
    "v_exp_f32 v161, v161\n"
    "v_smfmac_f32_32x32x32_bf16 v[80:95], v[224:227], v[184:191], v240\n"
    "v_exp_f32 v162, v162\n"
    "v_exp_f32 v163, v163\n"
    "v_exp_f32 v164, v164\n"
    "v_mfma_f32_32x32x16_bf16 v[176:191], v[216:219], v[156:159], 0\n"
    "v_exp_f32 v165, v165\n"
    "v_exp_f32 v166, v166\n"
    "v_exp_f32 v167, v167\n"
    "v_exp_f32 v168, v168\n"
    "v_exp_f32 v169, v169\n"
    "v_exp_f32 v170, v170\n"
    "v_smfmac_f32_32x32x32_bf16 v[96:111], v[220:223], v[160:167], v240\n"
    "v_exp_f32 v171, v171\n"
    "v_exp_f32 v172, v172\n"
    "v_exp_f32 v173, v173\n"
    "v_exp_f32 v174, v174\n"
    "v_exp_f32 v175, v175\n"
    "v_exp_f32 v176, v176\n"
    "v_exp_f32 v177, v177\n"
    "v_smfmac_f32_32x32x32_bf16 v[96:111], v[224:227], v[168:175], v240\n"
    "v_exp_f32 v178, v178\n"
    "v_exp_f32 v179, v179\n"
    "v_exp_f32 v180, v180\n"
    "s_waitcnt vmcnt(9)\n"
    "v_mfma_f32_32x32x16_bf16 v[160:175], v[228:231], v[128:131], 0\n"
    "v_exp_f32 v181, v181\n"
    "v_exp_f32 v182, v182\n"
    "v_exp_f32 v183, v183\n"
    "v_exp_f32 v184, v184\n"
    "v_exp_f32 v185, v185\n"
    "v_exp_f32 v186, v186\n"
    "v_smfmac_f32_32x32x32_bf16 v[112:127], v[220:223], v[176:183], v240\n"
    "v_exp_f32 v187, v187\n"
    "v_exp_f32 v188, v188\n"
    "v_exp_f32 v189, v189\n"
    "v_exp_f32 v190, v190\n"
    "v_exp_f32 v191, v191\n"
    "v_exp_f32 v160, v160\n"
    "v_exp_f32 v161, v161\n"
    "v_smfmac_f32_32x32x32_bf16 v[112:127], v[224:227], v[184:191], v240\n"
    "v_exp_f32 v162, v162\n"
    "v_exp_f32 v163, v163\n"
    "v_exp_f32 v164, v164\n"
    "v_mfma_f32_32x32x16_bf16 v[176:191], v[228:231], v[132:135], 0\n"
    "buffer_load_dwordx4 v[216:219], v242, s[32:35], s68 offen sc1\n"
    "s_mov_b64 exec, s[76:77]\n"
    "buffer_load_dwordx4 v[220:223], v241, s[32:35], s68 offen offset:1024 sc1\n"
    "buffer_load_dwordx4 v[224:227], v241, s[32:35], s68 offen offset:1536 sc1\n"
    "buffer_load_dword v251, v248, s[36:39], 0 offen\n"
    "v_add_u32 v248, s73, v248\n"
    "s_mov_b64 exec, -1\n"
    "v_exp_f32 v165, v165\n"
    "v_exp_f32 v166, v166\n"
    "v_exp_f32 v167, v167\n"
    "v_exp_f32 v168, v168\n"
    "v_exp_f32 v169, v169\n"
    "v_exp_f32 v170, v170\n"
    "v_smfmac_f32_32x32x32_bf16 v[0:15], v[232:235], v[160:167], v240\n"
    "v_exp_f32 v171, v171\n"
    "v_exp_f32 v172, v172\n"
    "v_exp_f32 v173, v173\n"
    "v_exp_f32 v174, v174\n"
    "v_exp_f32 v175, v175\n"
    "v_exp_f32 v176, v176\n"
    "v_exp_f32 v177, v177\n"
    "v_smfmac_f32_32x32x32_bf16 v[0:15], v[236:239], v[168:175], v240\n"
    "v_exp_f32 v178, v178\n"
    "v_exp_f32 v179, v179\n"
    "v_exp_f32 v180, v180\n"
    "v_mfma_f32_32x32x16_bf16 v[160:175], v[228:231], v[136:139], 0\n"
    "v_exp_f32 v181, v181\n"
    "v_exp_f32 v182, v182\n"
    "v_exp_f32 v183, v183\n"
    "v_exp_f32 v184, v184\n"
    "v_exp_f32 v185, v185\n"
    "v_exp_f32 v186, v186\n"
    "v_smfmac_f32_32x32x32_bf16 v[16:31], v[232:235], v[176:183], v240\n"
    "v_exp_f32 v187, v187\n"
    "v_exp_f32 v188, v188\n"
    "v_exp_f32 v189, v189\n"
    "v_exp_f32 v190, v190\n"
    "v_exp_f32 v191, v191\n"
    "v_exp_f32 v160, v160\n"
    "v_exp_f32 v161, v161\n"
    "v_smfmac_f32_32x32x32_bf16 v[16:31], v[236:239], v[184:191], v240\n"
    "v_exp_f32 v162, v162\n"
    "v_exp_f32 v163, v163\n"
    "v_exp_f32 v164, v164\n"
    "v_mfma_f32_32x32x16_bf16 v[176:191], v[228:231], v[140:143], 0\n"
    "v_exp_f32 v165, v165\n"
    "v_exp_f32 v166, v166\n"
    "v_exp_f32 v167, v167\n"
    "v_exp_f32 v168, v168\n"
    "v_exp_f32 v169, v169\n"
    "v_exp_f32 v170, v170\n"
    "v_smfmac_f32_32x32x32_bf16 v[32:47], v[232:235], v[160:167], v240\n"
    "v_exp_f32 v171, v171\n"
    "v_exp_f32 v172, v172\n"
    "v_exp_f32 v173, v173\n"
    "v_exp_f32 v174, v174\n"
    "v_exp_f32 v175, v175\n"
    "v_exp_f32 v176, v176\n"
    "v_exp_f32 v177, v177\n"
    "v_smfmac_f32_32x32x32_bf16 v[32:47], v[236:239], v[168:175], v240\n"
    "v_exp_f32 v178, v178\n"
    "v_exp_f32 v179, v179\n"
    "v_exp_f32 v180, v180\n"
    "v_mfma_f32_32x32x16_bf16 v[160:175], v[228:231], v[144:147], 0\n"
    "v_exp_f32 v181, v181\n"
    "v_exp_f32 v182, v182\n"
    "v_exp_f32 v183, v183\n"
    "v_exp_f32 v184, v184\n"
    "v_exp_f32 v185, v185\n"
    "v_exp_f32 v186, v186\n"
    "v_smfmac_f32_32x32x32_bf16 v[48:63], v[232:235], v[176:183], v240\n"
    "v_exp_f32 v187, v187\n"
    "v_exp_f32 v188, v188\n"
    "v_exp_f32 v189, v189\n"
    "v_exp_f32 v190, v190\n"
    "v_exp_f32 v191, v191\n"
    "v_exp_f32 v160, v160\n"
    "v_exp_f32 v161, v161\n"
    "v_smfmac_f32_32x32x32_bf16 v[48:63], v[236:239], v[184:191], v240\n"
    "v_exp_f32 v162, v162\n"
    "v_exp_f32 v163, v163\n"
    "v_exp_f32 v164, v164\n"
    "v_mfma_f32_32x32x16_bf16 v[176:191], v[228:231], v[148:151], 0\n"
    "v_exp_f32 v165, v165\n"
    "v_exp_f32 v166, v166\n"
    "v_exp_f32 v167, v167\n"
    "v_exp_f32 v168, v168\n"
    "v_exp_f32 v169, v169\n"
    "v_exp_f32 v170, v170\n"
    "v_smfmac_f32_32x32x32_bf16 v[64:79], v[232:235], v[160:167], v240\n"
    "v_exp_f32 v171, v171\n"
    "v_exp_f32 v172, v172\n"
    "v_exp_f32 v173, v173\n"
    "v_exp_f32 v174, v174\n"
    "v_exp_f32 v175, v175\n"
    "v_exp_f32 v176, v176\n"
    "v_exp_f32 v177, v177\n"
    "v_smfmac_f32_32x32x32_bf16 v[64:79], v[236:239], v[168:175], v240\n"
    "v_exp_f32 v178, v178\n"
    "v_exp_f32 v179, v179\n"
    "v_exp_f32 v180, v180\n"
    "v_mfma_f32_32x32x16_bf16 v[160:175], v[228:231], v[152:155], 0\n"
    "v_exp_f32 v181, v181\n"
    "v_exp_f32 v182, v182\n"
    "v_exp_f32 v183, v183\n"
    "v_exp_f32 v184, v184\n"
    "v_exp_f32 v185, v185\n"
    "v_exp_f32 v186, v186\n"
    "v_smfmac_f32_32x32x32_bf16 v[80:95], v[232:235], v[176:183], v240\n"
    "v_exp_f32 v187, v187\n"
    "v_exp_f32 v188, v188\n"
    "v_exp_f32 v189, v189\n"
    "v_exp_f32 v190, v190\n"
    "v_exp_f32 v191, v191\n"
    "v_exp_f32 v160, v160\n"
    "v_exp_f32 v161, v161\n"
    "v_smfmac_f32_32x32x32_bf16 v[80:95], v[236:239], v[184:191], v240\n"
    "v_exp_f32 v162, v162\n"
    "v_exp_f32 v163, v163\n"
    "v_exp_f32 v164, v164\n"
    "v_mfma_f32_32x32x16_bf16 v[176:191], v[228:231], v[156:159], 0\n"
    "v_exp_f32 v165, v165\n"
    "v_exp_f32 v166, v166\n"
    "v_exp_f32 v167, v167\n"
    "v_exp_f32 v168, v168\n"
    "v_exp_f32 v169, v169\n"
    "v_exp_f32 v170, v170\n"
    "v_smfmac_f32_32x32x32_bf16 v[96:111], v[232:235], v[160:167], v240\n"
    "v_exp_f32 v171, v171\n"
    "v_exp_f32 v172, v172\n"
    "v_exp_f32 v173, v173\n"
    "v_exp_f32 v174, v174\n"
    "v_exp_f32 v175, v175\n"
    "v_exp_f32 v176, v176\n"
    "v_exp_f32 v177, v177\n"
    "v_smfmac_f32_32x32x32_bf16 v[96:111], v[236:239], v[168:175], v240\n"
    "v_exp_f32 v178, v178\n"
    "v_exp_f32 v179, v179\n"
    "v_exp_f32 v180, v180\n"
    "s_waitcnt vmcnt(9)\n"
    "v_mfma_f32_32x32x16_bf16 v[160:175], v[192:195], v[128:131], 0\n"
    "v_exp_f32 v181, v181\n"
    "v_exp_f32 v182, v182\n"
    "v_exp_f32 v183, v183\n"
    "v_exp_f32 v184, v184\n"
    "v_exp_f32 v185, v185\n"
    "v_exp_f32 v186, v186\n"
    "v_smfmac_f32_32x32x32_bf16 v[112:127], v[232:235], v[176:183], v240\n"
    "v_exp_f32 v187, v187\n"
    "v_exp_f32 v188, v188\n"
    "v_exp_f32 v189, v189\n"
    "v_exp_f32 v190, v190\n"
    "v_exp_f32 v191, v191\n"
    "s_branch .La_loop\n"
    ".p2align 6\n"
    ".La_last:\n"
    "v_exp_f32 v160, v160\n"
    "v_exp_f32 v161, v161\n"
    "v_smfmac_f32_32x32x32_bf16 v[112:127], v[200:203], v[184:191], v240\n"
    "v_exp_f32 v162, v162\n"
    "v_exp_f32 v163, v163\n"
    "v_exp_f32 v164, v164\n"
    "v_mfma_f32_32x32x16_bf16 v[176:191], v[204:207], v[132:135], 0\n"
    "v_exp_f32 v165, v165\n"
    "v_exp_f32 v166, v166\n"
    "v_exp_f32 v167, v167\n"
    "v_exp_f32 v168, v168\n"
    "v_exp_f32 v169, v169\n"
    "v_exp_f32 v170, v170\n"
    "v_smfmac_f32_32x32x32_bf16 v[0:15], v[208:211], v[160:167], v240\n"
    "v_exp_f32 v171, v171\n"
    "v_exp_f32 v172, v172\n"
    "v_exp_f32 v173, v173\n"
    "v_exp_f32 v174, v174\n"
    "v_exp_f32 v175, v175\n"
    "v_exp_f32 v176, v176\n"
    "v_exp_f32 v177, v177\n"
    "v_smfmac_f32_32x32x32_bf16 v[0:15], v[212:215], v[168:175], v240\n"
    "v_exp_f32 v178, v178\n"
    "v_exp_f32 v179, v179\n"
    "v_exp_f32 v180, v180\n"
    "v_mfma_f32_32x32x16_bf16 v[160:175], v[204:207], v[136:139], 0\n"
    "v_exp_f32 v181, v181\n"
    "v_exp_f32 v182, v182\n"
    "v_exp_f32 v183, v183\n"
    "v_exp_f32 v184, v184\n"
    "v_exp_f32 v185, v185\n"
    "v_exp_f32 v186, v186\n"
    "v_smfmac_f32_32x32x32_bf16 v[16:31], v[208:211], v[176:183], v240\n"
    "v_exp_f32 v187, v187\n"
    "v_exp_f32 v188, v188\n"
    "v_exp_f32 v189, v189\n"
    "v_exp_f32 v190, v190\n"
    "v_exp_f32 v191, v191\n"
    "v_exp_f32 v160, v160\n"
    "v_exp_f32 v161, v161\n"
    "v_smfmac_f32_32x32x32_bf16 v[16:31], v[212:215], v[184:191], v240\n"
    "v_exp_f32 v162, v162\n"
    "v_exp_f32 v163, v163\n"
    "v_exp_f32 v164, v164\n"
    "v_mfma_f32_32x32x16_bf16 v[176:191], v[204:207], v[140:143], 0\n"
    "v_exp_f32 v165, v165\n"
    "v_exp_f32 v166, v166\n"
    "v_exp_f32 v167, v167\n"
    "v_exp_f32 v168, v168\n"
    "v_exp_f32 v169, v169\n"
    "v_exp_f32 v170, v170\n"
    "v_smfmac_f32_32x32x32_bf16 v[32:47], v[208:211], v[160:167], v240\n"
    "v_exp_f32 v171, v171\n"
    "v_exp_f32 v172, v172\n"
    "v_exp_f32 v173, v173\n"
    "v_exp_f32 v174, v174\n"
    "v_exp_f32 v175, v175\n"
    "v_exp_f32 v176, v176\n"
    "v_exp_f32 v177, v177\n"
    "v_smfmac_f32_32x32x32_bf16 v[32:47], v[212:215], v[168:175], v240\n"
    "v_exp_f32 v178, v178\n"
    "v_exp_f32 v179, v179\n"
    "v_exp_f32 v180, v180\n"
    "v_mfma_f32_32x32x16_bf16 v[160:175], v[204:207], v[144:147], 0\n"
    "v_exp_f32 v181, v181\n"
    "v_exp_f32 v182, v182\n"
    "v_exp_f32 v183, v183\n"
    "v_exp_f32 v184, v184\n"
    "v_exp_f32 v185, v185\n"
    "v_exp_f32 v186, v186\n"
    "v_smfmac_f32_32x32x32_bf16 v[48:63], v[208:211], v[176:183], v240\n"
    "v_exp_f32 v187, v187\n"
    "v_exp_f32 v188, v188\n"
    "v_exp_f32 v189, v189\n"
    "v_exp_f32 v190, v190\n"
    "v_exp_f32 v191, v191\n"
    "v_exp_f32 v160, v160\n"
    "v_exp_f32 v161, v161\n"
    "v_smfmac_f32_32x32x32_bf16 v[48:63], v[212:215], v[184:191], v240\n"
    "v_exp_f32 v162, v162\n"
    "v_exp_f32 v163, v163\n"
    "v_exp_f32 v164, v164\n"
    "v_mfma_f32_32x32x16_bf16 v[176:191], v[204:207], v[148:151], 0\n"
    "v_exp_f32 v165, v165\n"
    "v_exp_f32 v166, v166\n"
    "v_exp_f32 v167, v167\n"
    "v_exp_f32 v168, v168\n"
    "v_exp_f32 v169, v169\n"
    "v_exp_f32 v170, v170\n"
    "v_smfmac_f32_32x32x32_bf16 v[64:79], v[208:211], v[160:167], v240\n"
    "v_exp_f32 v171, v171\n"
    "v_exp_f32 v172, v172\n"
    "v_exp_f32 v173, v173\n"
    "v_exp_f32 v174, v174\n"
    "v_exp_f32 v175, v175\n"
    "v_exp_f32 v176, v176\n"
    "v_exp_f32 v177, v177\n"
    "v_smfmac_f32_32x32x32_bf16 v[64:79], v[212:215], v[168:175], v240\n"
    "v_exp_f32 v178, v178\n"
    "v_exp_f32 v179, v179\n"
    "v_exp_f32 v180, v180\n"
    "v_mfma_f32_32x32x16_bf16 v[160:175], v[204:207], v[152:155], 0\n"
    "v_exp_f32 v181, v181\n"
    "v_exp_f32 v182, v182\n"
    "v_exp_f32 v183, v183\n"
    "v_exp_f32 v184, v184\n"
    "v_exp_f32 v185, v185\n"
    "v_exp_f32 v186, v186\n"
    "v_smfmac_f32_32x32x32_bf16 v[80:95], v[208:211], v[176:183], v240\n"
    "v_exp_f32 v187, v187\n"
    "v_exp_f32 v188, v188\n"
    "v_exp_f32 v189, v189\n"
    "v_exp_f32 v190, v190\n"
    "v_exp_f32 v191, v191\n"
    "v_exp_f32 v160, v160\n"
    "v_exp_f32 v161, v161\n"
    "v_smfmac_f32_32x32x32_bf16 v[80:95], v[212:215], v[184:191], v240\n"
    "v_exp_f32 v162, v162\n"
    "v_exp_f32 v163, v163\n"
    "v_exp_f32 v164, v164\n"
    "v_mfma_f32_32x32x16_bf16 v[176:191], v[204:207], v[156:159], 0\n"
    "v_exp_f32 v165, v165\n"
    "v_exp_f32 v166, v166\n"
    "v_exp_f32 v167, v167\n"
    "v_exp_f32 v168, v168\n"
    "v_exp_f32 v169, v169\n"
    "v_exp_f32 v170, v170\n"
    "v_smfmac_f32_32x32x32_bf16 v[96:111], v[208:211], v[160:167], v240\n"
    "v_exp_f32 v171, v171\n"
    "v_exp_f32 v172, v172\n"
    "v_exp_f32 v173, v173\n"
    "v_exp_f32 v174, v174\n"
    "v_exp_f32 v175, v175\n"
    "v_exp_f32 v176, v176\n"
    "v_exp_f32 v177, v177\n"
    "v_smfmac_f32_32x32x32_bf16 v[96:111], v[212:215], v[168:175], v240\n"
    "v_exp_f32 v178, v178\n"
    "v_exp_f32 v179, v179\n"
    "v_exp_f32 v180, v180\n"
    "s_waitcnt vmcnt(5)\n"
    "v_mfma_f32_32x32x16_bf16 v[160:175], v[216:219], v[128:131], 0\n"
    "v_exp_f32 v181, v181\n"
    "v_exp_f32 v182, v182\n"
    "v_exp_f32 v183, v183\n"
    "v_exp_f32 v184, v184\n"
    "v_exp_f32 v185, v185\n"
    "v_exp_f32 v186, v186\n"
    "v_smfmac_f32_32x32x32_bf16 v[112:127], v[208:211], v[176:183], v240\n"
    "v_exp_f32 v187, v187\n"
    "v_exp_f32 v188, v188\n"
    "v_exp_f32 v189, v189\n"
    "v_exp_f32 v190, v190\n"
    "v_exp_f32 v191, v191\n"
    "v_exp_f32 v160, v160\n"
    "v_exp_f32 v161, v161\n"
    "v_smfmac_f32_32x32x32_bf16 v[112:127], v[212:215], v[184:191], v240\n"
    "v_exp_f32 v162, v162\n"
    "v_exp_f32 v163, v163\n"
    "v_exp_f32 v164, v164\n"
    "s_waitcnt vmcnt(1)\n"
    "v_mfma_f32_32x32x16_bf16 v[176:191], v[228:231], v[128:131], 0\n"
    "ds_read_b32 v200, v247 offset:8\n"
    "v_add_u32 v192, s99, v242\n"
    "v_lshl_add_u32 v193, v243, 2, s99\n"
    "v_exp_f32 v165, v165\n"
    "v_exp_f32 v166, v166\n"
    "v_exp_f32 v167, v167\n"
    "v_exp_f32 v168, v168\n"
    "v_exp_f32 v169, v169\n"
    "v_exp_f32 v170, v170\n"
    "v_smfmac_f32_32x32x32_bf16 v[0:15], v[220:223], v[160:167], v240\n"
    "v_exp_f32 v171, v171\n"
    "v_exp_f32 v172, v172\n"
    "v_exp_f32 v173, v173\n"
    "v_exp_f32 v174, v174\n"
    "v_exp_f32 v175, v175\n"
    "v_exp_f32 v176, v176\n"
    "v_exp_f32 v177, v177\n"
    "v_smfmac_f32_32x32x32_bf16 v[0:15], v[224:227], v[168:175], v240\n"
    "v_exp_f32 v178, v178\n"
    "v_exp_f32 v179, v179\n"
    "v_exp_f32 v180, v180\n"
    "v_mfma_f32_32x32x16_bf16 v[160:175], v[216:219], v[136:139], 0\n"
    "s_waitcnt lgkmcnt(0)\n"
    "v_cmp_gt_u32 vcc, 4, v200\n"
    "s_cbranch_vccnz .La_xn_wait\n"
    ".La_xn_go:\n"
    "v_exp_f32 v181, v181\n"
    "v_exp_f32 v182, v182\n"
    "v_exp_f32 v183, v183\n"
    "v_exp_f32 v184, v184\n"
    "v_exp_f32 v185, v185\n"
    "v_exp_f32 v186, v186\n"
    "v_smfmac_f32_32x32x32_bf16 v[0:15], v[232:235], v[176:183], v240\n"
    "v_exp_f32 v187, v187\n"
    "v_exp_f32 v188, v188\n"
    "v_exp_f32 v189, v189\n"
    "v_exp_f32 v190, v190\n"
    "v_exp_f32 v191, v191\n"
    "v_exp_f32 v160, v160\n"
    "v_exp_f32 v161, v161\n"
    "v_smfmac_f32_32x32x32_bf16 v[0:15], v[236:239], v[184:191], v240\n"
    "v_exp_f32 v162, v162\n"
    "v_exp_f32 v163, v163\n"
    "v_exp_f32 v164, v164\n"
    "v_mfma_f32_32x32x16_bf16 v[176:191], v[228:231], v[136:139], 0\n"
    "v_exp_f32 v165, v165\n"
    "v_exp_f32 v166, v166\n"
    "v_exp_f32 v167, v167\n"
    "v_exp_f32 v168, v168\n"
    "v_exp_f32 v169, v169\n"
    "v_exp_f32 v170, v170\n"
    "v_smfmac_f32_32x32x32_bf16 v[32:47], v[220:223], v[160:167], v240\n"
    "v_exp_f32 v171, v171\n"
    "v_exp_f32 v172, v172\n"
    "v_exp_f32 v173, v173\n"
    "v_exp_f32 v174, v174\n"
    "v_exp_f32 v175, v175\n"
    "ds_write_b128 v192, v[0:3]\n"
    "ds_write_b128 v192, v[4:7] offset:1024\n"
    "ds_write_b32 v193, v8 offset:2048\n"
    "v_exp_f32 v176, v176\n"
    "v_exp_f32 v177, v177\n"
    "v_smfmac_f32_32x32x32_bf16 v[32:47], v[224:227], v[168:175], v240\n"
    "v_exp_f32 v178, v178\n"
    "v_exp_f32 v179, v179\n"
    "v_exp_f32 v180, v180\n"
    "v_mfma_f32_32x32x16_bf16 v[160:175], v[216:219], v[144:147], 0\n"
    "v_add_u32 v194, s96, v242\n"
    "v_lshl_add_u32 v197, v243, 2, s96\n"
    "v_add_u32 v195, s97, v242\n"
    "v_lshl_add_u32 v198, v243, 2, s97\n"
    "v_add_u32 v196, s98, v242\n"
    "v_lshl_add_u32 v199, v243, 2, s98\n"
    "v_exp_f32 v181, v181\n"
    "v_exp_f32 v182, v182\n"
    "v_exp_f32 v183, v183\n"
    "v_exp_f32 v184, v184\n"
    "v_exp_f32 v185, v185\n"
    "v_exp_f32 v186, v186\n"
    "v_smfmac_f32_32x32x32_bf16 v[32:47], v[232:235], v[176:183], v240\n"
    "v_exp_f32 v187, v187\n"
    "v_exp_f32 v188, v188\n"
    "v_exp_f32 v189, v189\n"
    "v_exp_f32 v190, v190\n"
    "v_exp_f32 v191, v191\n"
    "v_exp_f32 v160, v160\n"
    "v_exp_f32 v161, v161\n"
    "v_smfmac_f32_32x32x32_bf16 v[32:47], v[236:239], v[184:191], v240\n"
    "v_exp_f32 v162, v162\n"
    "v_exp_f32 v163, v163\n"
    "v_exp_f32 v164, v164\n"
    "v_mfma_f32_32x32x16_bf16 v[176:191], v[228:231], v[144:147], 0\n"
    "v_exp_f32 v165, v165\n"
    "v_exp_f32 v166, v166\n"
    "v_exp_f32 v167, v167\n"
    "v_exp_f32 v168, v168\n"
    "v_exp_f32 v169, v169\n"
    "v_exp_f32 v170, v170\n"
    "v_smfmac_f32_32x32x32_bf16 v[64:79], v[220:223], v[160:167], v240\n"
    "v_exp_f32 v171, v171\n"
    "v_exp_f32 v172, v172\n"
    "v_exp_f32 v173, v173\n"
    "v_exp_f32 v174, v174\n"
    "v_exp_f32 v175, v175\n"
    "ds_write_b128 v192, v[32:35] offset:4608\n"
    "ds_write_b128 v192, v[36:39] offset:5632\n"
    "ds_write_b32 v193, v40 offset:6656\n"
    "v_exp_f32 v176, v176\n"
    "v_exp_f32 v177, v177\n"
    "v_smfmac_f32_32x32x32_bf16 v[64:79], v[224:227], v[168:175], v240\n"
    "v_exp_f32 v178, v178\n"
    "v_exp_f32 v179, v179\n"
    "v_exp_f32 v180, v180\n"
    "v_mfma_f32_32x32x16_bf16 v[160:175], v[216:219], v[152:155], 0\n"
    "v_exp_f32 v181, v181\n"
    "v_exp_f32 v182, v182\n"
    "v_exp_f32 v183, v183\n"
    "v_exp_f32 v184, v184\n"
    "v_exp_f32 v185, v185\n"
    "v_exp_f32 v186, v186\n"
    "v_smfmac_f32_32x32x32_bf16 v[64:79], v[232:235], v[176:183], v240\n"
    "v_exp_f32 v187, v187\n"
    "v_exp_f32 v188, v188\n"
    "v_exp_f32 v189, v189\n"
    "v_exp_f32 v190, v190\n"
    "v_exp_f32 v191, v191\n"
    "v_exp_f32 v160, v160\n"
    "v_exp_f32 v161, v161\n"
    "v_smfmac_f32_32x32x32_bf16 v[64:79], v[236:239], v[184:191], v240\n"
    "v_exp_f32 v162, v162\n"
    "v_exp_f32 v163, v163\n"
    "v_exp_f32 v164, v164\n"
    "v_mfma_f32_32x32x16_bf16 v[176:191], v[228:231], v[152:155], 0\n"
    "v_exp_f32 v165, v165\n"
    "v_exp_f32 v166, v166\n"
    "v_exp_f32 v167, v167\n"
    "v_exp_f32 v168, v168\n"
    "v_exp_f32 v169, v169\n"
    "v_exp_f32 v170, v170\n"
    "v_smfmac_f32_32x32x32_bf16 v[96:111], v[220:223], v[160:167], v240\n"
    "v_exp_f32 v171, v171\n"
    "v_exp_f32 v172, v172\n"
    "v_exp_f32 v173, v173\n"
    "v_exp_f32 v174, v174\n"
    "v_exp_f32 v175, v175\n"
    "ds_write_b128 v192, v[64:67] offset:9216\n"
    "ds_write_b128 v192, v[68:71] offset:10240\n"
    "ds_write_b32 v193, v72 offset:11264\n"
    "v_exp_f32 v176, v176\n"
    "v_exp_f32 v177, v177\n"
    "v_smfmac_f32_32x32x32_bf16 v[96:111], v[224:227], v[168:175], v240\n"
    "v_exp_f32 v178, v178\n"
    "v_exp_f32 v179, v179\n"
    "v_exp_f32 v180, v180\n"
    "v_mfma_f32_32x32x16_bf16 v[160:175], v[216:219], v[132:135], 0\n"
    "s_waitcnt lgkmcnt(0)\n"
    "s_mov_b64 exec, 1\n"
    "ds_add_u32 v247, v250 offset:12\n"
    "s_mov_b64 exec, -1\n"
    "v_exp_f32 v181, v181\n"
    "v_exp_f32 v182, v182\n"
    "v_exp_f32 v183, v183\n"
    "v_exp_f32 v184, v184\n"
    "v_exp_f32 v185, v185\n"
    "v_exp_f32 v186, v186\n"
    "v_smfmac_f32_32x32x32_bf16 v[96:111], v[232:235], v[176:183], v240\n"
    "v_exp_f32 v187, v187\n"
    "v_exp_f32 v188, v188\n"
    "v_exp_f32 v189, v189\n"
    "v_exp_f32 v190, v190\n"
    "v_exp_f32 v191, v191\n"
    "v_exp_f32 v160, v160\n"
    "v_exp_f32 v161, v161\n"
    "v_smfmac_f32_32x32x32_bf16 v[96:111], v[236:239], v[184:191], v240\n"
    "v_exp_f32 v162, v162\n"
    "v_exp_f32 v163, v163\n"
    "v_exp_f32 v164, v164\n"
    "v_mfma_f32_32x32x16_bf16 v[176:191], v[228:231], v[132:135], 0\n"
    "v_exp_f32 v165, v165\n"
    "v_exp_f32 v166, v166\n"
    "v_exp_f32 v167, v167\n"
    "v_exp_f32 v168, v168\n"
    "v_exp_f32 v169, v169\n"
    "v_exp_f32 v170, v170\n"
    "v_smfmac_f32_32x32x32_bf16 v[16:31], v[220:223], v[160:167], v240\n"
    "v_exp_f32 v171, v171\n"
    "v_exp_f32 v172, v172\n"
    "v_exp_f32 v173, v173\n"
    "v_exp_f32 v174, v174\n"
    "v_exp_f32 v175, v175\n"
    "v_exp_f32 v176, v176\n"
    "v_exp_f32 v177, v177\n"
    "v_smfmac_f32_32x32x32_bf16 v[16:31], v[224:227], v[168:175], v240\n"
    "v_exp_f32 v178, v178\n"
    "v_exp_f32 v179, v179\n"
    "v_exp_f32 v180, v180\n"
    "v_mfma_f32_32x32x16_bf16 v[160:175], v[216:219], v[140:143], 0\n"
    "ds_read_b32 v200, v247 offset:12\n"
    "v_exp_f32 v181, v181\n"
    "v_exp_f32 v182, v182\n"
    "v_exp_f32 v183, v183\n"
    "v_exp_f32 v184, v184\n"
    "v_exp_f32 v185, v185\n"
    "v_exp_f32 v186, v186\n"
    "v_smfmac_f32_32x32x32_bf16 v[16:31], v[232:235], v[176:183], v240\n"
    "v_exp_f32 v187, v187\n"
    "v_exp_f32 v188, v188\n"
    "v_exp_f32 v189, v189\n"
    "v_exp_f32 v190, v190\n"
    "v_exp_f32 v191, v191\n"
    "v_exp_f32 v160, v160\n"
    "v_exp_f32 v161, v161\n"
    "v_smfmac_f32_32x32x32_bf16 v[16:31], v[236:239], v[184:191], v240\n"
    "v_exp_f32 v162, v162\n"
    "v_exp_f32 v163, v163\n"
    "v_exp_f32 v164, v164\n"
    "v_mfma_f32_32x32x16_bf16 v[176:191], v[228:231], v[140:143], 0\n"
    "s_waitcnt lgkmcnt(0)\n"
    "v_cmp_gt_u32 vcc, 4, v200\n"
    "s_cbranch_vccnz .La_a0_wait\n"
    ".La_a0_go:\n"
    "ds_read_b32 v8, v197 offset:2048\n"
    "ds_read_b128 v[0:3], v194\n"
    "ds_read_b128 v[4:7], v194 offset:1024\n"
    "ds_read_b32 v40, v198 offset:6656\n"
    "ds_read_b128 v[32:35], v195 offset:4608\n"
    "ds_read_b128 v[36:39], v195 offset:5632\n"
    "ds_read_b32 v72, v199 offset:11264\n"
    "ds_read_b128 v[64:67], v196 offset:9216\n"
    "ds_read_b128 v[68:71], v196 offset:10240\n"
    "v_exp_f32 v165, v165\n"
    "v_exp_f32 v166, v166\n"
    "v_exp_f32 v167, v167\n"
    "v_exp_f32 v168, v168\n"
    "v_exp_f32 v169, v169\n"
    "v_exp_f32 v170, v170\n"
    "v_smfmac_f32_32x32x32_bf16 v[48:63], v[220:223], v[160:167], v240\n"
    "v_exp_f32 v171, v171\n"
    "v_exp_f32 v172, v172\n"
    "v_exp_f32 v173, v173\n"
    "v_exp_f32 v174, v174\n"
    "v_exp_f32 v175, v175\n"
    "ds_write_b128 v192, v[16:19] offset:2304\n"
    "ds_write_b128 v192, v[20:23] offset:3328\n"
    "ds_write_b32 v193, v24 offset:4352\n"
    "v_exp_f32 v176, v176\n"
    "v_exp_f32 v177, v177\n"
    "v_smfmac_f32_32x32x32_bf16 v[48:63], v[224:227], v[168:175], v240\n"
    "v_exp_f32 v178, v178\n"
    "v_exp_f32 v179, v179\n"
    "v_exp_f32 v180, v180\n"
    "v_mfma_f32_32x32x16_bf16 v[160:175], v[216:219], v[148:151], 0\n"
    "s_waitcnt lgkmcnt(6)\n"
    "v_add_f32 v8, v8, v40\n"
    "v_add_f32 v0, v0, v32\n"
    "v_add_f32 v1, v1, v33\n"
    "v_add_f32 v2, v2, v34\n"
    "v_add_f32 v3, v3, v35\n"
    "v_add_f32 v4, v4, v36\n"
    "v_add_f32 v5, v5, v37\n"
    "v_add_f32 v6, v6, v38\n"
    "v_add_f32 v7, v7, v39\n"
    "s_waitcnt lgkmcnt(3)\n"
    "v_add_f32 v8, v8, v72\n"
    "v_add_f32 v0, v0, v64\n"
    "v_add_f32 v1, v1, v65\n"
    "v_add_f32 v2, v2, v66\n"
    "v_add_f32 v3, v3, v67\n"
    "v_add_f32 v4, v4, v68\n"
    "v_add_f32 v5, v5, v69\n"
    "v_add_f32 v6, v6, v70\n"
    "v_add_f32 v7, v7, v71\n"
    "v_add_f32 v104, v104, v8\n"
    "v_add_f32 v96, v96, v0\n"
    "v_cmp_lt_f32 vcc, 0x5d800000, v104\n"
    "v_add_f32 v97, v97, v1\n"
    "v_add_f32 v98, v98, v2\n"
    "s_cbranch_vccnz .La_bad0\n"
    "v_cmp_gt_f32 vcc, 0x2b800000, v104\n"
    "v_add_f32 v99, v99, v3\n"
    "v_add_f32 v100, v100, v4\n"
    "s_cbranch_vccnz .La_bad0\n"
    "v_add_f32 v101, v101, v5\n"
    "v_add_f32 v102, v102, v6\n"
    "v_add_f32 v103, v103, v7\n"
    "v_rcp_f32 v104, v104\n"
    "s_nop 0\n"
    "v_mul_f32 v104, v104, v245\n"
    "v_mul_f32 v96, v104, v96\n"
    "v_mul_f32 v97, v104, v97\n"
    "v_mul_f32 v98, v104, v98\n"
    "v_mul_f32 v99, v104, v99\n"
    "v_mul_f32 v100, v104, v100\n"
    "v_mul_f32 v101, v104, v101\n"
    "v_mul_f32 v102, v104, v102\n"
    "v_mul_f32 v103, v104, v103\n"
    "v_cvt_pk_bf16_f32 v204, v96, v97\n"
    "v_cvt_pk_bf16_f32 v205, v98, v99\n"
    "v_cvt_pk_bf16_f32 v206, v100, v101\n"
    "v_cvt_pk_bf16_f32 v207, v102, v103\n"
    "v_min_u32 v204, -2, v204\n"
    "v_min_u32 v205, -2, v205\n"
    "v_min_u32 v206, -2, v206\n"
    "v_min_u32 v207, -2, v207\n"
    "buffer_store_dwordx4 v[204:207], v242, s[28:31], s72 offen sc1\n"
    "s_mov_b64 exec, 1\n"
    "ds_add_u32 v247, v250 offset:16\n"
    "s_mov_b64 exec, -1\n"
    ".La_comb0:\n"
    "v_exp_f32 v181, v181\n"
    "v_exp_f32 v182, v182\n"
    "v_exp_f32 v183, v183\n"
    "v_exp_f32 v184, v184\n"
    "v_exp_f32 v185, v185\n"
    "v_exp_f32 v186, v186\n"
    "v_smfmac_f32_32x32x32_bf16 v[48:63], v[232:235], v[176:183], v240\n"
    "v_exp_f32 v187, v187\n"
    "v_exp_f32 v188, v188\n"
    "v_exp_f32 v189, v189\n"
    "v_exp_f32 v190, v190\n"
    "v_exp_f32 v191, v191\n"
    "v_exp_f32 v160, v160\n"
    "v_exp_f32 v161, v161\n"
    "v_smfmac_f32_32x32x32_bf16 v[48:63], v[236:239], v[184:191], v240\n"
    "v_exp_f32 v162, v162\n"
    "v_exp_f32 v163, v163\n"
    "v_exp_f32 v164, v164\n"
    "v_mfma_f32_32x32x16_bf16 v[176:191], v[228:231], v[148:151], 0\n"
    "v_exp_f32 v165, v165\n"
    "v_exp_f32 v166, v166\n"
    "v_exp_f32 v167, v167\n"
    "v_exp_f32 v168, v168\n"
    "v_exp_f32 v169, v169\n"
    "v_exp_f32 v170, v170\n"
    "v_smfmac_f32_32x32x32_bf16 v[80:95], v[220:223], v[160:167], v240\n"
    "v_exp_f32 v171, v171\n"
    "v_exp_f32 v172, v172\n"
    "v_exp_f32 v173, v173\n"
    "v_exp_f32 v174, v174\n"
    "v_exp_f32 v175, v175\n"
    "ds_write_b128 v192, v[48:51] offset:6912\n"
    "ds_write_b128 v192, v[52:55] offset:7936\n"
    "ds_write_b32 v193, v56 offset:8960\n"
    "v_exp_f32 v176, v176\n"
    "v_exp_f32 v177, v177\n"
    "v_smfmac_f32_32x32x32_bf16 v[80:95], v[224:227], v[168:175], v240\n"
    "v_exp_f32 v178, v178\n"
    "v_exp_f32 v179, v179\n"
    "v_exp_f32 v180, v180\n"
    "v_mfma_f32_32x32x16_bf16 v[160:175], v[216:219], v[156:159], 0\n"
    "v_exp_f32 v181, v181\n"
    "v_exp_f32 v182, v182\n"
    "v_exp_f32 v183, v183\n"
    "v_exp_f32 v184, v184\n"
    "v_exp_f32 v185, v185\n"
    "v_exp_f32 v186, v186\n"
    "v_smfmac_f32_32x32x32_bf16 v[80:95], v[232:235], v[176:183], v240\n"
    "v_exp_f32 v187, v187\n"
    "v_exp_f32 v188, v188\n"
    "v_exp_f32 v189, v189\n"
    "v_exp_f32 v190, v190\n"
    "v_exp_f32 v191, v191\n"
    "v_exp_f32 v160, v160\n"
    "v_exp_f32 v161, v161\n"
    "v_smfmac_f32_32x32x32_bf16 v[80:95], v[236:239], v[184:191], v240\n"
    "v_exp_f32 v162, v162\n"
    "v_exp_f32 v163, v163\n"
    "v_exp_f32 v164, v164\n"
    "v_mfma_f32_32x32x16_bf16 v[176:191], v[228:231], v[156:159], 0\n"
    "v_exp_f32 v165, v165\n"
    "v_exp_f32 v166, v166\n"
    "v_exp_f32 v167, v167\n"
    "v_exp_f32 v168, v168\n"
    "v_exp_f32 v169, v169\n"
    "v_exp_f32 v170, v170\n"
    "v_smfmac_f32_32x32x32_bf16 v[112:127], v[220:223], v[160:167], v240\n"
    "v_exp_f32 v171, v171\n"
    "v_exp_f32 v172, v172\n"
    "v_exp_f32 v173, v173\n"
    "v_exp_f32 v174, v174\n"
    "v_exp_f32 v175, v175\n"
    "ds_write_b128 v192, v[80:83] offset:11520\n"
    "ds_write_b128 v192, v[84:87] offset:12544\n"
    "ds_write_b32 v193, v88 offset:13568\n"
    "v_exp_f32 v176, v176\n"
    "v_exp_f32 v177, v177\n"
    "v_smfmac_f32_32x32x32_bf16 v[112:127], v[224:227], v[168:175], v240\n"
    "v_exp_f32 v178, v178\n"
    "v_exp_f32 v179, v179\n"
    "v_exp_f32 v180, v180\n"
    "v_exp_f32 v181, v181\n"
    "v_exp_f32 v182, v182\n"
    "s_setprio 3\n"
    "s_waitcnt lgkmcnt(0)\n"
    "s_mov_b64 exec, 1\n"
    "ds_add_u32 v247, v250 offset:4\n"
    "s_mov_b64 exec, -1\n"
    "ds_read_b32 v200, v247 offset:4\n"
    "v_exp_f32 v183, v183\n"
    "v_exp_f32 v184, v184\n"
    "v_exp_f32 v185, v185\n"
    "v_exp_f32 v186, v186\n"
    "v_smfmac_f32_32x32x32_bf16 v[112:127], v[232:235], v[176:183], v240\n"
    "v_exp_f32 v187, v187\n"
    "v_exp_f32 v188, v188\n"
    "v_exp_f32 v189, v189\n"
    "s_waitcnt lgkmcnt(0)\n"
    "v_cmp_gt_u32 vcc, 4, v200\n"
    "s_cbranch_vccnz .La_a1_wait\n"
    ".La_a1_go:\n"
    "ds_read_b32 v24, v197 offset:4352\n"
    "ds_read_b128 v[16:19], v194 offset:2304\n"
    "ds_read_b128 v[20:23], v194 offset:3328\n"
    "ds_read_b32 v56, v198 offset:8960\n"
    "ds_read_b128 v[48:51], v195 offset:6912\n"
    "ds_read_b128 v[52:55], v195 offset:7936\n"
    "ds_read_b32 v88, v199 offset:13568\n"
    "ds_read_b128 v[80:83], v196 offset:11520\n"
    "ds_read_b128 v[84:87], v196 offset:12544\n"
    "v_exp_f32 v190, v190\n"
    "v_exp_f32 v191, v191\n"
    "s_nop 1\n"
    "v_smfmac_f32_32x32x32_bf16 v[112:127], v[236:239], v[184:191], v240\n"
    "s_waitcnt lgkmcnt(3)\n"
    "v_add_f32 v24, v24, v56\n"
    "v_add_f32 v16, v16, v48\n"
    "v_add_f32 v17, v17, v49\n"
    "v_add_f32 v18, v18, v50\n"
    "v_add_f32 v19, v19, v51\n"
    "v_add_f32 v20, v20, v52\n"
    "v_add_f32 v21, v21, v53\n"
    "v_add_f32 v22, v22, v54\n"
    "v_add_f32 v23, v23, v55\n"
    "s_waitcnt lgkmcnt(0)\n"
    "v_add_f32 v24, v24, v88\n"
    "v_add_f32 v16, v16, v80\n"
    "v_add_f32 v17, v17, v81\n"
    "v_add_f32 v18, v18, v82\n"
    "v_add_f32 v19, v19, v83\n"
    "v_add_f32 v20, v20, v84\n"
    "v_add_f32 v21, v21, v85\n"
    "v_add_f32 v22, v22, v86\n"
    "v_add_f32 v23, v23, v87\n"
    "v_add_f32 v120, v120, v24\n"
    "v_add_f32 v112, v112, v16\n"
    "v_cmp_lt_f32 vcc, 0x5d800000, v120\n"
    "v_add_f32 v113, v113, v17\n"
    "v_add_f32 v114, v114, v18\n"
    "s_cbranch_vccnz .La_bad1\n"
    "v_cmp_gt_f32 vcc, 0x2b800000, v120\n"
    "v_add_f32 v115, v115, v19\n"
    "v_add_f32 v116, v116, v20\n"
    "s_cbranch_vccnz .La_bad1\n"
    "v_add_f32 v117, v117, v21\n"
    "v_add_f32 v118, v118, v22\n"
    "v_add_f32 v119, v119, v23\n"
    "v_rcp_f32 v120, v120\n"
    "s_nop 0\n"
    "v_mul_f32 v120, v120, v246\n"
    "v_mul_f32 v112, v120, v112\n"
    "v_mul_f32 v113, v120, v113\n"
    "v_mul_f32 v114, v120, v114\n"
    "v_mul_f32 v115, v120, v115\n"
    "v_mul_f32 v116, v120, v116\n"
    "v_mul_f32 v117, v120, v117\n"
    "v_mul_f32 v118, v120, v118\n"
    "v_mul_f32 v119, v120, v119\n"
    "v_cvt_pk_bf16_f32 v208, v112, v113\n"
    "v_cvt_pk_bf16_f32 v209, v114, v115\n"
    "v_cvt_pk_bf16_f32 v210, v116, v117\n"
    "v_cvt_pk_bf16_f32 v211, v118, v119\n"
    "v_min_u32 v208, -2, v208\n"
    "v_min_u32 v209, -2, v209\n"
    "v_min_u32 v210, -2, v210\n"
    "v_min_u32 v211, -2, v211\n"
    "buffer_store_dwordx4 v[208:211], v242, s[28:31], s72 offen offset:1024 sc1\n"
    "s_mov_b64 exec, 1\n"
    "ds_add_u32 v247, v250 offset:40\n"
    "s_mov_b64 exec, -1\n"
    ".La_comb1:\n"
    "s_cmp_lg_u32 s78, 0\n"
    "s_cbranch_scc1 .La_slow\n"
    ".La_done:\n"
    "s_or_b32 s58, s79, s54\n"
    "s_cbranch_scc1 .Lfinal\n"
    "v_mov_b32 v248, s56\n"
    "v_mov_b32 v249, s57\n"
    "s_mov_b64 exec, 1\n"
    "buffer_store_dwordx2 v[248:249], off, s[28:31], s58 sc1\n"
    "s_mov_b64 exec, -1\n"
    "s_branch .Lfinal\n"
    ".La_xn_wait:\n"
    "s_mov_b32 s71, 8388608\n"
    ".Lxn2148_spin:\n"
    "ds_read_b32 v200, v247 offset:8\n"
    "s_waitcnt lgkmcnt(0)\n"
    "v_cmp_gt_u32 vcc, 4, v200\n"
    "s_cbranch_vccz .Lxn2148_go\n"
    "s_sleep 1\n"
    "s_sub_u32 s71, s71, 1\n"
    "s_cbranch_scc0 .Lxn2148_spin\n"
    ".Lxn2148_go:\n"
    "s_branch .La_xn_go\n"
    ".La_a0_wait:\n"
    "s_mov_b32 s71, 8388608\n"
    ".La02160_spin:\n"
    "ds_read_b32 v200, v247 offset:12\n"
    "s_waitcnt lgkmcnt(0)\n"
    "v_cmp_gt_u32 vcc, 4, v200\n"
    "s_cbranch_vccz .La02160_go\n"
    "s_sub_u32 s71, s71, 1\n"
    "s_cbranch_scc0 .La02160_spin\n"
    ".La02160_go:\n"
    "s_branch .La_a0_go\n"
    ".La_a1_wait:\n"
    "s_mov_b32 s71, 8388608\n"
    ".La12171_spin:\n"
    "ds_read_b32 v200, v247 offset:4\n"
    "s_waitcnt lgkmcnt(0)\n"
    "v_cmp_gt_u32 vcc, 4, v200\n"
    "s_cbranch_vccz .La12171_go\n"
    "s_sub_u32 s71, s71, 1\n"
    "s_cbranch_scc0 .La12171_spin\n"
    ".La12171_go:\n"
    "s_branch .La_a1_go\n"
    ".La_bad0:\n"
    "s_or_b32 s78, s78, 1\n"
    "s_branch .La_comb0\n"
    ".La_bad1:\n"
    "s_or_b32 s78, s78, 2\n"
    "s_branch .La_comb1\n"
    ".La_slow:\n"
    "s_setprio 0\n"
    "s_waitcnt vmcnt(0) lgkmcnt(0)\n"
    "v_mov_b64 v[96:97], 0\n"
    "v_mov_b64 v[98:99], 0\n"
    "v_mov_b64 v[100:101], 0\n"
    "v_mov_b64 v[102:103], 0\n"
    "v_mov_b64 v[104:105], 0\n"
    "v_mov_b64 v[106:107], 0\n"
    "v_mov_b64 v[108:109], 0\n"
    "v_mov_b64 v[110:111], 0\n"
    "v_mov_b64 v[112:113], 0\n"
    "v_mov_b64 v[114:115], 0\n"
    "v_mov_b64 v[116:117], 0\n"
    "v_mov_b64 v[118:119], 0\n"
    "v_mov_b64 v[120:121], 0\n"
    "v_mov_b64 v[122:123], 0\n"
    "v_mov_b64 v[124:125], 0\n"
    "v_mov_b64 v[126:127], 0\n"
    "v_mov_b32 v176, 0xff800000\n"
    "v_mov_b32 v177, 0xff800000\n"
    "s_mov_b32 s68, 262144\n"
    "s_mov_b32 s71, 63\n"
    ".La_slow1:\n"
    "buffer_load_dwordx4 v[228:231], v242, s[28:31], s68 offen sc1\n"
    "s_mov_b64 exec, s[76:77]\n"
    "buffer_load_dwordx4 v[232:235], v241, s[28:31], s68 offen offset:1024 sc1\n"
    "buffer_load_dwordx4 v[236:239], v241, s[28:31], s68 offen offset:1536 sc1\n"
    "s_mov_b64 exec, -1\n"
    "s_add_u32 s68, s68, 2048\n"
    "s_waitcnt vmcnt(0)\n"
    "v_mfma_f32_32x32x16_bf16 v[160:175], v[228:231], v[152:155], 0\n"
    "s_nop 11\n"
    "v_max3_f32 v176, v160, v161, v176\n"
    "v_max3_f32 v176, v162, v163, v176\n"
    "v_max3_f32 v176, v164, v165, v176\n"
    "v_max3_f32 v176, v166, v167, v176\n"
    "v_max3_f32 v176, v168, v169, v176\n"
    "v_max3_f32 v176, v170, v171, v176\n"
    "v_max3_f32 v176, v172, v173, v176\n"
    "v_max3_f32 v176, v174, v175, v176\n"
    "v_mfma_f32_32x32x16_bf16 v[160:175], v[228:231], v[156:159], 0\n"
    "s_nop 11\n"
    "v_max3_f32 v177, v160, v161, v177\n"
    "v_max3_f32 v177, v162, v163, v177\n"
    "v_max3_f32 v177, v164, v165, v177\n"
    "v_max3_f32 v177, v166, v167, v177\n"
    "v_max3_f32 v177, v168, v169, v177\n"
    "v_max3_f32 v177, v170, v171, v177\n"
    "v_max3_f32 v177, v172, v173, v177\n"
    "v_max3_f32 v177, v174, v175, v177\n"
    "s_sub_u32 s71, s71, 1\n"
    "s_cbranch_scc0 .La_slow1\n"
    "v_mov_b32 v251, v176\n"
    "s_nop 1\n"
    "v_permlane32_swap_b32 v251, v176\n"
    "v_max_f32 v176, v251, v176\n"
    "v_mov_b32 v251, v177\n"
    "s_nop 1\n"
    "v_permlane32_swap_b32 v251, v177\n"
    "v_max_f32 v177, v251, v177\n"
    "s_mov_b32 s68, 262144\n"
    "s_mov_b32 s71, 63\n"
    ".La_slow2:\n"
    "buffer_load_dwordx4 v[228:231], v242, s[28:31], s68 offen sc1\n"
    "s_mov_b64 exec, s[76:77]\n"
    "buffer_load_dwordx4 v[232:235], v241, s[28:31], s68 offen offset:1024 sc1\n"
    "buffer_load_dwordx4 v[236:239], v241, s[28:31], s68 offen offset:1536 sc1\n"
    "s_mov_b64 exec, -1\n"
    "s_add_u32 s68, s68, 2048\n"
    "s_waitcnt vmcnt(0)\n"
    "v_mfma_f32_32x32x16_bf16 v[160:175], v[228:231], v[152:155], 0\n"
    "s_nop 11\n"
    "v_sub_f32 v160, v160, v176\n"
    "v_sub_f32 v161, v161, v176\n"
    "v_sub_f32 v162, v162, v176\n"
    "v_sub_f32 v163, v163, v176\n"
    "v_sub_f32 v164, v164, v176\n"
    "v_sub_f32 v165, v165, v176\n"
    "v_sub_f32 v166, v166, v176\n"
    "v_sub_f32 v167, v167, v176\n"
    "v_sub_f32 v168, v168, v176\n"
    "v_sub_f32 v169, v169, v176\n"
    "v_sub_f32 v170, v170, v176\n"
    "v_sub_f32 v171, v171, v176\n"
    "v_sub_f32 v172, v172, v176\n"
    "v_sub_f32 v173, v173, v176\n"
    "v_sub_f32 v174, v174, v176\n"
    "v_sub_f32 v175, v175, v176\n"
    "v_exp_f32 v160, v160\n"
    "v_exp_f32 v161, v161\n"
    "v_exp_f32 v162, v162\n"
    "v_exp_f32 v163, v163\n"
    "v_exp_f32 v164, v164\n"
    "v_exp_f32 v165, v165\n"
    "v_exp_f32 v166, v166\n"
    "v_exp_f32 v167, v167\n"
    "v_exp_f32 v168, v168\n"
    "v_exp_f32 v169, v169\n"
    "v_exp_f32 v170, v170\n"
    "v_exp_f32 v171, v171\n"
    "v_exp_f32 v172, v172\n"
    "v_exp_f32 v173, v173\n"
    "v_exp_f32 v174, v174\n"
    "v_exp_f32 v175, v175\n"
    "v_smfmac_f32_32x32x32_bf16 v[96:111], v[232:235], v[160:167], v240\n"
    "s_nop 0\n"
    "v_smfmac_f32_32x32x32_bf16 v[96:111], v[236:239], v[168:175], v240\n"
    "v_mfma_f32_32x32x16_bf16 v[160:175], v[228:231], v[156:159], 0\n"
    "s_nop 11\n"
    "v_sub_f32 v160, v160, v177\n"
    "v_sub_f32 v161, v161, v177\n"
    "v_sub_f32 v162, v162, v177\n"
    "v_sub_f32 v163, v163, v177\n"
    "v_sub_f32 v164, v164, v177\n"
    "v_sub_f32 v165, v165, v177\n"
    "v_sub_f32 v166, v166, v177\n"
    "v_sub_f32 v167, v167, v177\n"
    "v_sub_f32 v168, v168, v177\n"
    "v_sub_f32 v169, v169, v177\n"
    "v_sub_f32 v170, v170, v177\n"
    "v_sub_f32 v171, v171, v177\n"
    "v_sub_f32 v172, v172, v177\n"
    "v_sub_f32 v173, v173, v177\n"
    "v_sub_f32 v174, v174, v177\n"
    "v_sub_f32 v175, v175, v177\n"
    "v_exp_f32 v160, v160\n"
    "v_exp_f32 v161, v161\n"
    "v_exp_f32 v162, v162\n"
    "v_exp_f32 v163, v163\n"
    "v_exp_f32 v164, v164\n"
    "v_exp_f32 v165, v165\n"
    "v_exp_f32 v166, v166\n"
    "v_exp_f32 v167, v167\n"
    "v_exp_f32 v168, v168\n"
    "v_exp_f32 v169, v169\n"
    "v_exp_f32 v170, v170\n"
    "v_exp_f32 v171, v171\n"
    "v_exp_f32 v172, v172\n"
    "v_exp_f32 v173, v173\n"
    "v_exp_f32 v174, v174\n"
    "v_exp_f32 v175, v175\n"
    "v_smfmac_f32_32x32x32_bf16 v[112:127], v[232:235], v[160:167], v240\n"
    "s_nop 0\n"
    "v_smfmac_f32_32x32x32_bf16 v[112:127], v[236:239], v[168:175], v240\n"
    "s_sub_u32 s71, s71, 1\n"
    "s_cbranch_scc0 .La_slow2\n"
    "s_nop 7\n"
    "s_nop 7\n"
    "s_bitcmp1_b32 s78, 0\n"
    "s_cbranch_scc0 .La_slow3\n"
    "v_rcp_f32 v104, v104\n"
    "s_nop 0\n"
    "v_mul_f32 v104, v104, v245\n"
    "v_mul_f32 v96, v104, v96\n"
    "v_mul_f32 v97, v104, v97\n"
    "v_mul_f32 v98, v104, v98\n"
    "v_mul_f32 v99, v104, v99\n"
    "v_mul_f32 v100, v104, v100\n"
    "v_mul_f32 v101, v104, v101\n"
    "v_mul_f32 v102, v104, v102\n"
    "v_mul_f32 v103, v104, v103\n"
    "v_cvt_pk_bf16_f32 v204, v96, v97\n"
    "v_cvt_pk_bf16_f32 v205, v98, v99\n"
    "v_cvt_pk_bf16_f32 v206, v100, v101\n"
    "v_cvt_pk_bf16_f32 v207, v102, v103\n"
    "v_min_u32 v204, -2, v204\n"
    "v_min_u32 v205, -2, v205\n"
    "v_min_u32 v206, -2, v206\n"
    "v_min_u32 v207, -2, v207\n"
    "buffer_store_dwordx4 v[204:207], v242, s[28:31], s72 offen sc1\n"
    "s_mov_b64 exec, 1\n"
    "ds_add_u32 v247, v250 offset:16\n"
    "s_mov_b64 exec, -1\n"
    ".La_slow3:\n"
    "s_bitcmp1_b32 s78, 1\n"
    "s_cbranch_scc0 .La_done\n"
    "v_rcp_f32 v120, v120\n"
    "s_nop 0\n"
    "v_mul_f32 v120, v120, v246\n"
    "v_mul_f32 v112, v120, v112\n"
    "v_mul_f32 v113, v120, v113\n"
    "v_mul_f32 v114, v120, v114\n"
    "v_mul_f32 v115, v120, v115\n"
    "v_mul_f32 v116, v120, v116\n"
    "v_mul_f32 v117, v120, v117\n"
    "v_mul_f32 v118, v120, v118\n"
    "v_mul_f32 v119, v120, v119\n"
    "v_cvt_pk_bf16_f32 v208, v112, v113\n"
    "v_cvt_pk_bf16_f32 v209, v114, v115\n"
    "v_cvt_pk_bf16_f32 v210, v116, v117\n"
    "v_cvt_pk_bf16_f32 v211, v118, v119\n"
    "v_min_u32 v208, -2, v208\n"
    "v_min_u32 v209, -2, v209\n"
    "v_min_u32 v210, -2, v210\n"
    "v_min_u32 v211, -2, v211\n"
    "buffer_store_dwordx4 v[208:211], v242, s[28:31], s72 offen offset:1024 sc1\n"
    "s_mov_b64 exec, 1\n"
    "ds_add_u32 v247, v250 offset:40\n"
    "s_mov_b64 exec, -1\n"
    "s_branch .La_done\n"
    ".Lgemm:\n"
    "s_cmp_eq_u32 s54, 5\n"
    "s_cbranch_scc0 .Lg_ncp\n"
    "s_getpc_b64 s[100:101]\n"
    ".Lpc2392:\n"
    "s_sub_u32 s48, s100, .Lpc2392-.Lstart\n"
    "s_subb_u32 s49, s101, 0\n"
    "s_and_b32 s49, s49, 0xffff\n"
    "s_mov_b32 s50, .Lend-.Lstart\n"
    "s_mov_b32 s51, 0x20000\n"
    "s_lshr_b32 s58, s79, 3\n"
    "s_lshl_b32 s58, s58, 7\n"
    "v_lshl_add_u32 v4, v243, 12, s58\n"
    "buffer_load_dword v227, v4, s[48:51], 0 offen\n"
    ".Lg_ncp:\n"
    "s_lshl_b32 s58, s55, 16\n"
    "s_lshl_b32 s59, s53, 7\n"
    "s_add_u32 s58, s58, s59\n"
    "v_lshl_add_u32 v4, v243, 10, s58\n"
    "s_lshl_b32 s58, s79, 11\n"
    "v_and_b32 v4, 15, v243\n"
    "v_lshl_add_u32 v4, v4, 7, s58\n"
    "s_waitcnt lgkmcnt(0)\n"
    "s_mov_b32 s40, s6\n"
    "s_and_b32 s41, s7, 0xffff\n"
    "s_mov_b32 s42, 524288\n"
    "s_mov_b32 s43, 0x20000\n"
    "s_cmp_eq_u32 s54, 4\n"
    "s_cbranch_scc0 .Lg_nw\n"
    "buffer_load_dword v227, v4, s[40:43], 0 offen\n"
    ".Lg_nw:\n"
    "s_barrier\n"
    "s_mov_b32 s28, s22\n"
    "s_and_b32 s29, s23, 0xffff\n"
    "s_mov_b32 s30, 10485760\n"
    "s_mov_b32 s31, 0x20000\n"
    "s_cmp_eq_u32 s54, 7\n"
    "s_cbranch_scc0 .Lg_st\n"
    "s_setprio 3\n"
    "s_bitcmp1_b32 s53, 0\n"
    "s_cselect_b64 s[44:45], s[10:11], s[8:9]\n"
    "s_mov_b32 s44, s44\n"
    "s_and_b32 s45, s45, 0xffff\n"
    "s_mov_b32 s46, 16384\n"
    "s_mov_b32 s47, 0x20000\n"
    "s_add_u32 s58, s55, 0\n"
    "s_and_b32 s58, s58, 3\n"
    "s_lshl_b32 s58, s58, 12\n"
    "s_add_u32 m0, s58, 131072\n"
    "s_nop 0\n"
    "buffer_load_dwordx4 v242, s[44:47], s58 offen lds\n"
    "buffer_load_dwordx4 v242, s[44:47], s58 offen offset:1024 lds\n"
    "buffer_load_dwordx4 v242, s[44:47], s58 offen offset:2048 lds\n"
    "buffer_load_dwordx4 v242, s[44:47], s58 offen offset:3072 lds\n"
    "s_add_u32 s58, s55, 1\n"
    "s_and_b32 s58, s58, 3\n"
    "s_lshl_b32 s58, s58, 12\n"
    "s_add_u32 m0, s58, 131072\n"
    "s_nop 0\n"
    "buffer_load_dwordx4 v242, s[44:47], s58 offen lds\n"
    "buffer_load_dwordx4 v242, s[44:47], s58 offen offset:1024 lds\n"
    "buffer_load_dwordx4 v242, s[44:47], s58 offen offset:2048 lds\n"
    "buffer_load_dwordx4 v242, s[44:47], s58 offen offset:3072 lds\n"
    "s_add_u32 s58, s55, 2\n"
    "s_and_b32 s58, s58, 3\n"
    "s_lshl_b32 s58, s58, 12\n"
    "s_add_u32 m0, s58, 131072\n"
    "s_nop 0\n"
    "buffer_load_dwordx4 v242, s[44:47], s58 offen lds\n"
    "buffer_load_dwordx4 v242, s[44:47], s58 offen offset:1024 lds\n"
    "buffer_load_dwordx4 v242, s[44:47], s58 offen offset:2048 lds\n"
    "buffer_load_dwordx4 v242, s[44:47], s58 offen offset:3072 lds\n"
    "s_add_u32 s58, s55, 3\n"
    "s_and_b32 s58, s58, 3\n"
    "s_lshl_b32 s58, s58, 12\n"
    "s_add_u32 m0, s58, 131072\n"
    "s_nop 0\n"
    "buffer_load_dwordx4 v242, s[44:47], s58 offen lds\n"
    "buffer_load_dwordx4 v242, s[44:47], s58 offen offset:1024 lds\n"
    "buffer_load_dwordx4 v242, s[44:47], s58 offen offset:2048 lds\n"
    "buffer_load_dwordx4 v242, s[44:47], s58 offen offset:3072 lds\n"
    "buffer_load_dwordx2 v[224:225], off, s[28:31], 0 sc1\n"
    "s_setprio 0\n"
    ".Lg_st:\n"
    "v_lshrrev_b32 v1, 3, v243\n"
    "v_and_b32 v2, 7, v243\n"
    "v_lshlrev_b32 v3, 10, v1\n"
    "v_lshl_add_u32 v3, v2, 4, v3\n"
    "s_lshl_b32 s67, s52, 3\n"
    "s_add_u32 s67, s67, s54\n"
    "s_lshl_b32 s67, s67, 13\n"
    "s_lshl_b32 s66, s53, 7\n"
    "s_add_u32 s67, s67, s66\n"
    "v_add_u32 v244, s67, v3\n"
    "v_and_b32 v236, 31, v243\n"
    "v_lshrrev_b32 v237, 5, v243\n"
    "s_lshl_b32 s70, s75, 14\n"
    "s_mov_b32 s74, 114688\n"
    "s_add_u32 s70, s70, 65536\n"
    "v_bfe_u32 v4, v243, 4, 1\n"
    "v_lshlrev_b32 v5, 2, v4\n"
    "v_xor_b32 v5, v5, v2\n"
    "v_lshlrev_b32 v5, 4, v5\n"
    "v_lshl_add_u32 v6, v1, 10, v5\n"
    "v_add_u32 v234, s66, v6\n"
    "v_add_u32 v235, 7168, v234\n"
    "s_mov_b32 s24, s4\n"
    "s_and_b32 s25, s5, 0xffff\n"
    "s_mov_b32 s26, 2097152\n"
    "s_mov_b32 s27, 0x20000\n"
    "s_mov_b32 s48, s20\n"
    "s_and_b32 s49, s21, 0xffff\n"
    "s_mov_b32 s50, 2097152\n"
    "s_mov_b32 s51, 0x20000\n"
    "s_mov_b32 s32, s14\n"
    "s_and_b32 s33, s15, 0xffff\n"
    "s_mov_b32 s34, 1835008\n"
    "s_mov_b32 s35, 0x20000\n"
    "v_bfe_u32 v7, v243, 2, 2\n"
    "v_and_b32 v8, 3, v243\n"
    "v_lshlrev_b32 v9, 3, v8\n"
    "v_lshl_add_u32 v9, v4, 5, v9\n"
    "v_add_u32 v10, 0, v237\n"
    "v_xor_b32 v10, v10, v236\n"
    "v_lshlrev_b32 v10, 4, v10\n"
    "v_lshl_add_u32 v16, v237, 9, v10\n"
    "v_add_u32 v10, 2, v237\n"
    "v_xor_b32 v10, v10, v236\n"
    "v_lshlrev_b32 v10, 4, v10\n"
    "v_lshl_add_u32 v17, v237, 9, v10\n"
    "v_add_u32 v10, 4, v237\n"
    "v_xor_b32 v10, v10, v236\n"
    "v_lshlrev_b32 v10, 4, v10\n"
    "v_lshl_add_u32 v18, v237, 9, v10\n"
    "v_add_u32 v10, 6, v237\n"
    "v_xor_b32 v10, v10, v236\n"
    "v_lshlrev_b32 v10, 4, v10\n"
    "v_lshl_add_u32 v19, v237, 9, v10\n"
    "v_lshrrev_b32 v10, 1, v7\n"
    "v_lshl_add_u32 v10, v10, 6, v9\n"
    "v_lshl_add_u32 v10, v7, 7, v10\n"
    "v_lshl_add_u32 v245, v237, 9, v10\n"
    "v_lshl_add_u32 v11, v237, 10, v10\n"
    "v_lshrrev_b32 v12, 4, v243\n"
    "v_and_b32 v13, 15, v243\n"
    "v_xor_b32 v14, v12, v13\n"
    "v_lshlrev_b32 v14, 4, v14\n"
    "v_lshl_add_u32 v228, v12, 9, v14\n"
    "v_xor_b32 v229, 64, v228\n"
    "v_mov_b32 v240, 162304\n"
    "v_mov_b32 v241, 1\n"
    "s_mul_i32 s72, s52, 229376\n"
    "s_add_u32 s72, s72, 3145728\n"
    "s_lshl_b32 s73, s52, 8\n"
    "s_lshl_b32 s58, s53, 3\n"
    "s_add_u32 s73, s73, s58\n"
    "s_mov_b32 s68, 0\n"
    "s_mul_i32 s58, s53, 448\n"
    "s_cmp_eq_u32 s54, 7\n"
    "s_cbranch_scc1 .Lg3\n"
    "s_mov_b32 s36, s16\n"
    "s_and_b32 s37, s17, 0xffff\n"
    "s_mov_b32 s38, 1835008\n"
    "s_mov_b32 s39, 0x20000\n"
    "s_and_b32 s59, s53, 1\n"
    "s_lshl_b32 s59, s59, 6\n"
    "s_add_u32 s58, s58, s59\n"
    "s_lshl_b32 s59, s75, 7\n"
    "s_add_u32 s58, s58, s59\n"
    "v_mul_u32_u24 v6, 3584, v1\n"
    "v_lshlrev_b32 v14, 1, v4\n"
    "v_xor_b32 v14, v14, v2\n"
    "v_lshlrev_b32 v14, 4, v14\n"
    "v_add3_u32 v238, v14, v6, s58\n"
    "v_xor_b32 v14, 64, v238\n"
    "v_add_u32 v239, 27648, v14\n"
    "v_add_u32 v222, 55296, v238\n"
    "v_add_u32 v223, 82944, v14\n"
    "v_lshrrev_b32 v14, 1, v7\n"
    "v_lshl_add_u32 v14, v4, 1, v14\n"
    "v_lshlrev_b32 v14, 5, v14\n"
    "v_lshl_add_u32 v14, v8, 3, v14\n"
    "v_lshl_add_u32 v14, v7, 7, v14\n"
    "v_lshl_add_u32 v14, v12, 10, v14\n"
    "v_add_u32 v230, s70, v14\n"
    "v_xor_b32 v231, 32, v230\n"
    "v_xor_b32 v232, 64, v230\n"
    "v_xor_b32 v233, 96, v230\n"
    "s_lshr_b32 s64, s75, 1\n"
    "s_lshl_b32 s65, s64, 6\n"
    "s_add_u32 s65, s65, 65536\n"
    "v_xor_b32 v196, s65, v11\n"
    "s_and_b32 s65, s75, 1\n"
    "s_lshl_b32 s67, s65, 15\n"
    "v_add_u32 v192, s67, v16\n"
    "v_add_u32 v193, s67, v17\n"
    "v_add_u32 v194, s67, v18\n"
    "v_add_u32 v195, s67, v19\n"
    "s_lshl_b32 s67, s65, 7\n"
    "s_add_u32 s67, s67, 161792\n"
    "v_lshl_add_u32 v197, v236, 2, s67\n"
    "s_lshl_b32 s65, s65, 10\n"
    "s_lshl_b32 s64, s64, 12\n"
    "s_add_u32 s65, s65, s64\n"
    "s_add_u32 s65, s65, 147456\n"
    "v_add_u32 v198, s65, v242\n"
    "v_mov_b64 v[0:1], 0\n"
    "v_mov_b64 v[2:3], 0\n"
    "v_mov_b64 v[4:5], 0\n"
    "v_mov_b64 v[6:7], 0\n"
    "v_mov_b64 v[8:9], 0\n"
    "v_mov_b64 v[10:11], 0\n"
    "v_mov_b64 v[12:13], 0\n"
    "v_mov_b64 v[14:15], 0\n"
    "v_mov_b64 v[16:17], 0\n"
    "v_mov_b64 v[18:19], 0\n"
    "v_mov_b64 v[20:21], 0\n"
    "v_mov_b64 v[22:23], 0\n"
    "v_mov_b64 v[24:25], 0\n"
    "v_mov_b64 v[26:27], 0\n"
    "v_mov_b64 v[28:29], 0\n"
    "v_mov_b64 v[30:31], 0\n"
    "v_mov_b64 v[32:33], 0\n"
    "v_mov_b64 v[34:35], 0\n"
    "v_mov_b64 v[36:37], 0\n"
    "v_mov_b64 v[38:39], 0\n"
    "v_mov_b64 v[40:41], 0\n"
    "v_mov_b64 v[42:43], 0\n"
    "v_mov_b64 v[44:45], 0\n"
    "v_mov_b64 v[46:47], 0\n"
    "v_mov_b64 v[48:49], 0\n"
    "v_mov_b64 v[50:51], 0\n"
    "v_mov_b64 v[52:53], 0\n"
    "v_mov_b64 v[54:55], 0\n"
    "v_mov_b64 v[56:57], 0\n"
    "v_mov_b64 v[58:59], 0\n"
    "v_mov_b64 v[60:61], 0\n"
    "v_mov_b64 v[62:63], 0\n"
    "v_mov_b64 v[64:65], 0\n"
    "v_mov_b64 v[66:67], 0\n"
    "v_mov_b64 v[68:69], 0\n"
    "v_mov_b64 v[70:71], 0\n"
    "v_mov_b64 v[72:73], 0\n"
    "v_mov_b64 v[74:75], 0\n"
    "v_mov_b64 v[76:77], 0\n"
    "v_mov_b64 v[78:79], 0\n"
    "v_mov_b64 v[80:81], 0\n"
    "v_mov_b64 v[82:83], 0\n"
    "v_mov_b64 v[84:85], 0\n"
    "v_mov_b64 v[86:87], 0\n"
    "v_mov_b64 v[88:89], 0\n"
    "v_mov_b64 v[90:91], 0\n"
    "v_mov_b64 v[92:93], 0\n"
    "v_mov_b64 v[94:95], 0\n"
    "v_mov_b64 v[96:97], 0\n"
    "v_mov_b64 v[98:99], 0\n"
    "v_mov_b64 v[100:101], 0\n"
    "v_mov_b64 v[102:103], 0\n"
    "v_mov_b64 v[104:105], 0\n"
    "v_mov_b64 v[106:107], 0\n"
    "v_mov_b64 v[108:109], 0\n"
    "v_mov_b64 v[110:111], 0\n"
    "v_mov_b64 v[112:113], 0\n"
    "v_mov_b64 v[114:115], 0\n"
    "v_mov_b64 v[116:117], 0\n"
    "v_mov_b64 v[118:119], 0\n"
    "v_mov_b64 v[120:121], 0\n"
    "v_mov_b64 v[122:123], 0\n"
    "v_mov_b64 v[124:125], 0\n"
    "v_mov_b64 v[126:127], 0\n"
    "s_mul_i32 s59, s75, 11\n"
    "s_lshl_b32 s60, s55, 2\n"
    "s_add_u32 s59, s59, s60\n"
    "s_add_u32 s61, s59, 0\n"
    "s_and_b32 s61, s61, 31\n"
    "s_lshl_b32 s62, s61, 11\n"
    "s_add_u32 m0, s62, 65536\n"
    "s_lshl_b32 s61, s61, 14\n"
    "buffer_load_dwordx4 v234, s[40:43], s61 offen lds\n"
    "buffer_load_dwordx4 v235, s[40:43], s61 offen offset:1024 lds\n"
    "s_add_u32 s61, s59, 1\n"
    "s_and_b32 s61, s61, 31\n"
    "s_lshl_b32 s62, s61, 11\n"
    "s_add_u32 m0, s62, 65536\n"
    "s_lshl_b32 s61, s61, 14\n"
    "buffer_load_dwordx4 v234, s[40:43], s61 offen lds\n"
    "buffer_load_dwordx4 v235, s[40:43], s61 offen offset:1024 lds\n"
    "s_add_u32 s61, s59, 2\n"
    "s_and_b32 s61, s61, 31\n"
    "s_lshl_b32 s62, s61, 11\n"
    "s_add_u32 m0, s62, 65536\n"
    "s_lshl_b32 s61, s61, 14\n"
    "buffer_load_dwordx4 v234, s[40:43], s61 offen lds\n"
    "buffer_load_dwordx4 v235, s[40:43], s61 offen offset:1024 lds\n"
    "s_add_u32 s61, s59, 3\n"
    "s_and_b32 s61, s61, 31\n"
    "s_lshl_b32 s62, s61, 11\n"
    "s_add_u32 m0, s62, 65536\n"
    "s_lshl_b32 s61, s61, 14\n"
    "buffer_load_dwordx4 v234, s[40:43], s61 offen lds\n"
    "buffer_load_dwordx4 v235, s[40:43], s61 offen offset:1024 lds\n"
    "s_add_u32 s61, s59, 4\n"
    "s_and_b32 s61, s61, 31\n"
    "s_lshl_b32 s62, s61, 11\n"
    "s_add_u32 m0, s62, 65536\n"
    "s_lshl_b32 s61, s61, 14\n"
    "buffer_load_dwordx4 v234, s[40:43], s61 offen lds\n"
    "buffer_load_dwordx4 v235, s[40:43], s61 offen offset:1024 lds\n"
    "s_add_u32 s61, s59, 5\n"
    "s_and_b32 s61, s61, 31\n"
    "s_lshl_b32 s62, s61, 11\n"
    "s_add_u32 m0, s62, 65536\n"
    "s_lshl_b32 s61, s61, 14\n"
    "buffer_load_dwordx4 v234, s[40:43], s61 offen lds\n"
    "buffer_load_dwordx4 v235, s[40:43], s61 offen offset:1024 lds\n"
    "s_add_u32 s61, s59, 6\n"
    "s_and_b32 s61, s61, 31\n"
    "s_lshl_b32 s62, s61, 11\n"
    "s_add_u32 m0, s62, 65536\n"
    "s_lshl_b32 s61, s61, 14\n"
    "buffer_load_dwordx4 v234, s[40:43], s61 offen lds\n"
    "buffer_load_dwordx4 v235, s[40:43], s61 offen offset:1024 lds\n"
    "s_add_u32 s61, s59, 7\n"
    "s_and_b32 s61, s61, 31\n"
    "s_lshl_b32 s62, s61, 11\n"
    "s_add_u32 m0, s62, 65536\n"
    "s_lshl_b32 s61, s61, 14\n"
    "buffer_load_dwordx4 v234, s[40:43], s61 offen lds\n"
    "buffer_load_dwordx4 v235, s[40:43], s61 offen offset:1024 lds\n"
    "s_add_u32 s61, s59, 8\n"
    "s_and_b32 s61, s61, 31\n"
    "s_lshl_b32 s62, s61, 11\n"
    "s_add_u32 m0, s62, 65536\n"
    "s_lshl_b32 s61, s61, 14\n"
    "buffer_load_dwordx4 v234, s[40:43], s61 offen lds\n"
    "buffer_load_dwordx4 v235, s[40:43], s61 offen offset:1024 lds\n"
    "s_add_u32 s61, s59, 9\n"
    "s_and_b32 s61, s61, 31\n"
    "s_lshl_b32 s62, s61, 11\n"
    "s_add_u32 m0, s62, 65536\n"
    "s_lshl_b32 s61, s61, 14\n"
    "buffer_load_dwordx4 v234, s[40:43], s61 offen lds\n"
    "buffer_load_dwordx4 v235, s[40:43], s61 offen offset:1024 lds\n"
    "s_add_u32 s61, s59, 10\n"
    "s_and_b32 s61, s61, 31\n"
    "s_lshl_b32 s62, s61, 11\n"
    "s_add_u32 m0, s62, 65536\n"
    "s_lshl_b32 s61, s61, 14\n"
    "buffer_load_dwordx4 v234, s[40:43], s61 offen lds\n"
    "buffer_load_dwordx4 v235, s[40:43], s61 offen offset:1024 lds\n"
    "s_cmp_eq_u32 s54, 4\n"
    "s_cselect_b64 s[44:45], s[14:15], s[16:17]\n"
    "s_mov_b32 s44, s44\n"
    "s_and_b32 s45, s45, 0xffff\n"
    "s_mov_b32 s46, 1835008\n"
    "s_mov_b32 s47, 0x20000\n"
    "s_cmp_eq_u32 s54, 6\n"
    "s_cselect_b32 s46, 0, s46\n"
    "s_mul_i32 s58, s79, 1024\n"
    "v_lshl_add_u32 v221, v243, 7, s58\n"
    "s_mov_b32 exec_lo, 0xff\n"
    "s_mov_b32 exec_hi, 0x0\n"
    "buffer_load_dword v220, v221, s[44:47], 0 offen\n"
    "s_mov_b64 exec, -1\n"
    "s_mov_b32 s40, s18\n"
    "s_and_b32 s41, s19, 0xffff\n"
    "s_mov_b32 s42, 1835008\n"
    "s_mov_b32 s43, 0x20000\n"
    "s_mov_b32 s44, s12\n"
    "s_and_b32 s45, s13, 0xffff\n"
    "s_mov_b32 s46, 524288\n"
    "s_mov_b32 s47, 0x20000\n"
    "s_mov_b32 s69, 1835008\n"
    "s_mul_i32 s64, s75, 3\n"
    "s_bitcmp1_b32 s75, 0\n"
    "s_cselect_b32 s62, 12288, 0\n"
    "s_and_b32 s67, s53, 3\n"
    "s_mul_i32 s67, s67, 2\n"
    "s_add_u32 s67, s67, s55\n"
    "s_sub_u32 s67, s67, s53\n"
    "s_add_u32 s67, s67, 7\n"
    "s_and_b32 s67, s67, 7\n"
    "s_cmp_eq_u32 s67, 7\n"
    "s_cselect_b32 s67, 0, s67\n"
    "s_add_u32 s65, s67, 1\n"
    "s_cmp_gt_u32 s65, 7\n"
    "s_addc_u32 s65, s65, s53\n"
    "s_and_b32 s65, s65, 7\n"
    "s_and_b32 s66, s65, 1\n"
    "s_mul_i32 s65, s65, 7\n"
    "s_add_u32 s65, s65, s66\n"
    "s_lshl_b32 s65, s65, 1\n"
    "s_add_u32 s65, s65, s64\n"
    "s_lshl_b32 s4, s65, 14\n"
    "s_sub_u32 s4, s4, s62\n"
    "s_lshl_b32 s65, s65, 11\n"
    "s_add_u32 s12, s65, s72\n"
    "s_add_u32 s65, s67, 2\n"
    "s_cmp_gt_u32 s65, 7\n"
    "s_addc_u32 s65, s65, s53\n"
    "s_and_b32 s65, s65, 7\n"
    "s_and_b32 s66, s65, 1\n"
    "s_mul_i32 s65, s65, 7\n"
    "s_add_u32 s65, s65, s66\n"
    "s_lshl_b32 s65, s65, 1\n"
    "s_add_u32 s65, s65, s64\n"
    "s_lshl_b32 s5, s65, 14\n"
    "s_sub_u32 s5, s5, s62\n"
    "s_lshl_b32 s65, s65, 11\n"
    "s_add_u32 s13, s65, s72\n"
    "s_add_u32 s65, s67, 3\n"
    "s_cmp_gt_u32 s65, 7\n"
    "s_addc_u32 s65, s65, s53\n"
    "s_and_b32 s65, s65, 7\n"
    "s_and_b32 s66, s65, 1\n"
    "s_mul_i32 s65, s65, 7\n"
    "s_add_u32 s65, s65, s66\n"
    "s_lshl_b32 s65, s65, 1\n"
    "s_add_u32 s65, s65, s64\n"
    "s_lshl_b32 s6, s65, 14\n"
    "s_sub_u32 s6, s6, s62\n"
    "s_lshl_b32 s65, s65, 11\n"
    "s_add_u32 s14, s65, s72\n"
    "s_add_u32 s65, s67, 4\n"
    "s_cmp_gt_u32 s65, 7\n"
    "s_addc_u32 s65, s65, s53\n"
    "s_and_b32 s65, s65, 7\n"
    "s_and_b32 s66, s65, 1\n"
    "s_mul_i32 s65, s65, 7\n"
    "s_add_u32 s65, s65, s66\n"
    "s_lshl_b32 s65, s65, 1\n"
    "s_add_u32 s65, s65, s64\n"
    "s_lshl_b32 s7, s65, 14\n"
    "s_sub_u32 s7, s7, s62\n"
    "s_lshl_b32 s65, s65, 11\n"
    "s_add_u32 s15, s65, s72\n"
    "s_add_u32 s65, s67, 5\n"
    "s_cmp_gt_u32 s65, 7\n"
    "s_addc_u32 s65, s65, s53\n"
    "s_and_b32 s65, s65, 7\n"
    "s_and_b32 s66, s65, 1\n"
    "s_mul_i32 s65, s65, 7\n"
    "s_add_u32 s65, s65, s66\n"
    "s_lshl_b32 s65, s65, 1\n"
    "s_add_u32 s65, s65, s64\n"
    "s_lshl_b32 s8, s65, 14\n"
    "s_sub_u32 s8, s8, s62\n"
    "s_lshl_b32 s65, s65, 11\n"
    "s_add_u32 s16, s65, s72\n"
    "s_add_u32 s65, s67, 6\n"
    "s_cmp_gt_u32 s65, 7\n"
    "s_addc_u32 s65, s65, s53\n"
    "s_and_b32 s65, s65, 7\n"
    "s_and_b32 s66, s65, 1\n"
    "s_mul_i32 s65, s65, 7\n"
    "s_add_u32 s65, s65, s66\n"
    "s_lshl_b32 s65, s65, 1\n"
    "s_add_u32 s65, s65, s64\n"
    "s_lshl_b32 s9, s65, 14\n"
    "s_sub_u32 s9, s9, s62\n"
    "s_lshl_b32 s65, s65, 11\n"
    "s_add_u32 s17, s65, s72\n"
    "s_add_u32 s65, s67, 7\n"
    "s_cmp_gt_u32 s65, 7\n"
    "s_addc_u32 s65, s65, s53\n"
    "s_and_b32 s65, s65, 7\n"
    "s_and_b32 s66, s65, 1\n"
    "s_mul_i32 s65, s65, 7\n"
    "s_add_u32 s65, s65, s66\n"
    "s_lshl_b32 s65, s65, 1\n"
    "s_add_u32 s65, s65, s64\n"
    "s_lshl_b32 s10, s65, 14\n"
    "s_sub_u32 s10, s10, s62\n"
    "s_lshl_b32 s65, s65, 11\n"
    "s_add_u32 s18, s65, s72\n"
    "s_lshl_b32 s62, s62, 1\n"
    "s_add_u32 s62, s62, 4096\n"
    "s_lshl_b32 s23, s52, 2\n"
    "s_add_u32 s23, s23, s75\n"
    "s_lshl_b32 s23, s23, 14\n"
    "s_add_u32 s23, s23, 1048576\n"
    "s_lshl_b32 s78, s52, 1\n"
    "s_and_b32 s64, s79, 1\n"
    "s_add_u32 s78, s78, s64\n"
    "s_lshl_b32 s78, s78, 7\n"
    "s_lshl_b32 s11, s52, 3\n"
    "s_add_u32 s11, s11, s53\n"
    "s_lshl_b32 s11, s11, 2\n"
    "s_add_u32 s11, s11, s75\n"
    "s_lshl_b32 s11, s11, 11\n"
    "s_add_u32 s11, s11, 1048576\n"
    "s_lshl_b32 s76, s75, 13\n"
    "s_add_u32 s76, s76, 131072\n"
    "s_waitcnt vmcnt(1)\n"
    "v_lshlrev_b32 v227, 7, v243\n"
    "v_and_b32 v226, 7, v243\n"
    "v_max_u32 v226, 1, v226\n"
    "v_xor_b32 v226, s53, v226\n"
    "v_lshlrev_b32 v226, 3, v226\n"
    "v_mov_b64 v[248:249], -1\n"
    "v_mov_b64 v[250:251], -1\n"
    "s_barrier\n"
    "ds_read_b64 v[224:225], v240 offset:48\n"
    "buffer_load_dwordx4 v[252:255], v244, s[24:27], 0 offen\n"
    "ds_read_b32 v246, v197\n"
    "ds_read_b128 v[128:131], v192\n"
    "ds_read_b64_tr_b16 v[176:177], v196\n"
    "ds_read_b64_tr_b16 v[178:179], v196 offset:512\n"
    "ds_read_b128 v[132:135], v193 offset:1024\n"
    "ds_read_b64_tr_b16 v[180:181], v196 offset:2048\n"
    "ds_read_b64_tr_b16 v[182:183], v196 offset:2560\n"
    "ds_read_b128 v[136:139], v194 offset:2048\n"
    "ds_read_b64_tr_b16 v[184:185], v196 offset:4096\n"
    "ds_read_b64_tr_b16 v[186:187], v196 offset:4608\n"
    "ds_read_b128 v[140:143], v195 offset:3072\n"
    "ds_read_b64_tr_b16 v[188:189], v196 offset:6144\n"
    "ds_read_b64_tr_b16 v[190:191], v196 offset:6656\n"
    "s_waitcnt lgkmcnt(9)\n"
    "v_mfma_f32_32x32x16_bf16 v[160:175], v[176:179], v[128:131], 0\n"
    "ds_read_b128 v[128:131], v192 offset:4096\n"
    "ds_read_b64_tr_b16 v[176:177], v196 offset:8192\n"
    "ds_read_b64_tr_b16 v[178:179], v196 offset:8704\n"
    "s_waitcnt lgkmcnt(9)\n"
    "v_mfma_f32_32x32x16_bf16 v[160:175], v[180:183], v[132:135], v[160:175]\n"
    "ds_read_b128 v[132:135], v193 offset:5120\n"
    "ds_read_b64_tr_b16 v[180:181], v196 offset:10240\n"
    "ds_read_b64_tr_b16 v[182:183], v196 offset:10752\n"
    "s_waitcnt lgkmcnt(9)\n"
    "v_mfma_f32_32x32x16_bf16 v[160:175], v[184:187], v[136:139], v[160:175]\n"
    "ds_read_b128 v[136:139], v194 offset:6144\n"
    "ds_read_b64_tr_b16 v[184:185], v196 offset:12288\n"
    "ds_read_b64_tr_b16 v[186:187], v196 offset:12800\n"
    "s_waitcnt lgkmcnt(9)\n"
    "v_mfma_f32_32x32x16_bf16 v[160:175], v[188:191], v[140:143], v[160:175]\n"
    "ds_read_b128 v[140:143], v195 offset:7168\n"
    "ds_read_b64_tr_b16 v[188:189], v196 offset:14336\n"
    "ds_read_b64_tr_b16 v[190:191], v196 offset:14848\n"
    "s_waitcnt lgkmcnt(9)\n"
    "v_mfma_f32_32x32x16_bf16 v[160:175], v[176:179], v[128:131], v[160:175]\n"
    "ds_read_b128 v[128:131], v192 offset:8192\n"
    "ds_read_b64_tr_b16 v[176:177], v196 offset:16384\n"
    "ds_read_b64_tr_b16 v[178:179], v196 offset:16896\n"
    "s_waitcnt lgkmcnt(9)\n"
    "v_mfma_f32_32x32x16_bf16 v[160:175], v[180:183], v[132:135], v[160:175]\n"
    "ds_read_b128 v[132:135], v193 offset:9216\n"
    "ds_read_b64_tr_b16 v[180:181], v196 offset:18432\n"
    "ds_read_b64_tr_b16 v[182:183], v196 offset:18944\n"
    "s_waitcnt lgkmcnt(9)\n"
    "v_mfma_f32_32x32x16_bf16 v[160:175], v[184:187], v[136:139], v[160:175]\n"
    "ds_read_b128 v[136:139], v194 offset:10240\n"
    "ds_read_b64_tr_b16 v[184:185], v196 offset:20480\n"
    "ds_read_b64_tr_b16 v[186:187], v196 offset:20992\n"
    "s_waitcnt lgkmcnt(9)\n"
    "v_mfma_f32_32x32x16_bf16 v[160:175], v[188:191], v[140:143], v[160:175]\n"
    "ds_read_b128 v[140:143], v195 offset:11264\n"
    "ds_read_b64_tr_b16 v[188:189], v196 offset:22528\n"
    "ds_read_b64_tr_b16 v[190:191], v196 offset:23040\n"
    "s_waitcnt lgkmcnt(9)\n"
    "v_mfma_f32_32x32x16_bf16 v[160:175], v[176:179], v[128:131], v[160:175]\n"
    "ds_read_b128 v[128:131], v192 offset:12288\n"
    "ds_read_b64_tr_b16 v[176:177], v196 offset:24576\n"
    "ds_read_b64_tr_b16 v[178:179], v196 offset:25088\n"
    "s_waitcnt lgkmcnt(9)\n"
    "v_mfma_f32_32x32x16_bf16 v[160:175], v[180:183], v[132:135], v[160:175]\n"
    "ds_read_b128 v[132:135], v193 offset:13312\n"
    "ds_read_b64_tr_b16 v[180:181], v196 offset:26624\n"
    "ds_read_b64_tr_b16 v[182:183], v196 offset:27136\n"
    "s_waitcnt lgkmcnt(9)\n"
    "v_mfma_f32_32x32x16_bf16 v[160:175], v[184:187], v[136:139], v[160:175]\n"
    "ds_read_b128 v[136:139], v194 offset:14336\n"
    "ds_read_b64_tr_b16 v[184:185], v196 offset:28672\n"
    "ds_read_b64_tr_b16 v[186:187], v196 offset:29184\n"
    "s_waitcnt lgkmcnt(9)\n"
    "v_mfma_f32_32x32x16_bf16 v[160:175], v[188:191], v[140:143], v[160:175]\n"
    "ds_read_b128 v[140:143], v195 offset:15360\n"
    "ds_read_b64_tr_b16 v[188:189], v196 offset:30720\n"
    "ds_read_b64_tr_b16 v[190:191], v196 offset:31232\n"
    "s_waitcnt lgkmcnt(9)\n"
    "v_mfma_f32_32x32x16_bf16 v[160:175], v[176:179], v[128:131], v[160:175]\n"
    "ds_read_b128 v[128:131], v192 offset:16384\n"
    "ds_read_b64_tr_b16 v[176:177], v196 offset:32768\n"
    "ds_read_b64_tr_b16 v[178:179], v196 offset:33280\n"
    "s_waitcnt lgkmcnt(9)\n"
    "v_mfma_f32_32x32x16_bf16 v[160:175], v[180:183], v[132:135], v[160:175]\n"
    "ds_read_b128 v[132:135], v193 offset:17408\n"
    "ds_read_b64_tr_b16 v[180:181], v196 offset:34816\n"
    "ds_read_b64_tr_b16 v[182:183], v196 offset:35328\n"
    "s_waitcnt lgkmcnt(9)\n"
    "v_mfma_f32_32x32x16_bf16 v[160:175], v[184:187], v[136:139], v[160:175]\n"
    "ds_read_b128 v[136:139], v194 offset:18432\n"
    "ds_read_b64_tr_b16 v[184:185], v196 offset:36864\n"
    "ds_read_b64_tr_b16 v[186:187], v196 offset:37376\n"
    "s_waitcnt lgkmcnt(9)\n"
    "v_mfma_f32_32x32x16_bf16 v[160:175], v[188:191], v[140:143], v[160:175]\n"
    "ds_read_b128 v[140:143], v195 offset:19456\n"
    "ds_read_b64_tr_b16 v[188:189], v196 offset:38912\n"
    "ds_read_b64_tr_b16 v[190:191], v196 offset:39424\n"
    "s_waitcnt lgkmcnt(9)\n"
    "v_mfma_f32_32x32x16_bf16 v[160:175], v[176:179], v[128:131], v[160:175]\n"
    "ds_read_b128 v[128:131], v192 offset:20480\n"
    "ds_read_b64_tr_b16 v[176:177], v196 offset:40960\n"
    "ds_read_b64_tr_b16 v[178:179], v196 offset:41472\n"
    "s_waitcnt lgkmcnt(9)\n"
    "v_mfma_f32_32x32x16_bf16 v[160:175], v[180:183], v[132:135], v[160:175]\n"
    "ds_read_b128 v[132:135], v193 offset:21504\n"
    "ds_read_b64_tr_b16 v[180:181], v196 offset:43008\n"
    "ds_read_b64_tr_b16 v[182:183], v196 offset:43520\n"
    "s_waitcnt lgkmcnt(9)\n"
    "v_mfma_f32_32x32x16_bf16 v[160:175], v[184:187], v[136:139], v[160:175]\n"
    "ds_read_b128 v[136:139], v194 offset:22528\n"
    "ds_read_b64_tr_b16 v[184:185], v196 offset:45056\n"
    "ds_read_b64_tr_b16 v[186:187], v196 offset:45568\n"
    "s_waitcnt lgkmcnt(9)\n"
    "v_mfma_f32_32x32x16_bf16 v[160:175], v[188:191], v[140:143], v[160:175]\n"
    "ds_read_b128 v[140:143], v195 offset:23552\n"
    "ds_read_b64_tr_b16 v[188:189], v196 offset:47104\n"
    "ds_read_b64_tr_b16 v[190:191], v196 offset:47616\n"
    "s_waitcnt lgkmcnt(9)\n"
    "v_mfma_f32_32x32x16_bf16 v[160:175], v[176:179], v[128:131], v[160:175]\n"
    "ds_read_b128 v[128:131], v192 offset:24576\n"
    "ds_read_b64_tr_b16 v[176:177], v196 offset:49152\n"
    "ds_read_b64_tr_b16 v[178:179], v196 offset:49664\n"
    "s_waitcnt lgkmcnt(9)\n"
    "v_mfma_f32_32x32x16_bf16 v[160:175], v[180:183], v[132:135], v[160:175]\n"
    "ds_read_b128 v[132:135], v193 offset:25600\n"
    "ds_read_b64_tr_b16 v[180:181], v196 offset:51200\n"
    "ds_read_b64_tr_b16 v[182:183], v196 offset:51712\n"
    "s_waitcnt lgkmcnt(9)\n"
    "v_mfma_f32_32x32x16_bf16 v[160:175], v[184:187], v[136:139], v[160:175]\n"
    "ds_read_b128 v[136:139], v194 offset:26624\n"
    "ds_read_b64_tr_b16 v[184:185], v196 offset:53248\n"
    "ds_read_b64_tr_b16 v[186:187], v196 offset:53760\n"
    "s_waitcnt lgkmcnt(9)\n"
    "v_mfma_f32_32x32x16_bf16 v[160:175], v[188:191], v[140:143], v[160:175]\n"
    "ds_read_b128 v[140:143], v195 offset:27648\n"
    "ds_read_b64_tr_b16 v[188:189], v196 offset:55296\n"
    "ds_read_b64_tr_b16 v[190:191], v196 offset:55808\n"
    "s_waitcnt lgkmcnt(9)\n"
    "v_mfma_f32_32x32x16_bf16 v[160:175], v[176:179], v[128:131], v[160:175]\n"
    "ds_read_b128 v[128:131], v192 offset:28672\n"
    "ds_read_b64_tr_b16 v[176:177], v196 offset:57344\n"
    "ds_read_b64_tr_b16 v[178:179], v196 offset:57856\n"
    "s_waitcnt lgkmcnt(9)\n"
    "v_mfma_f32_32x32x16_bf16 v[160:175], v[180:183], v[132:135], v[160:175]\n"
    "ds_read_b128 v[132:135], v193 offset:29696\n"
    "ds_read_b64_tr_b16 v[180:181], v196 offset:59392\n"
    "ds_read_b64_tr_b16 v[182:183], v196 offset:59904\n"
    "s_waitcnt lgkmcnt(9)\n"
    "v_mfma_f32_32x32x16_bf16 v[160:175], v[184:187], v[136:139], v[160:175]\n"
    "ds_read_b128 v[136:139], v194 offset:30720\n"
    "ds_read_b64_tr_b16 v[184:185], v196 offset:61440\n"
    "ds_read_b64_tr_b16 v[186:187], v196 offset:61952\n"
    "s_waitcnt lgkmcnt(9)\n"
    "v_mfma_f32_32x32x16_bf16 v[160:175], v[188:191], v[140:143], v[160:175]\n"
    "ds_read_b128 v[140:143], v195 offset:31744\n"
    "ds_read_b64_tr_b16 v[188:189], v196 offset:63488\n"
    "ds_read_b64_tr_b16 v[190:191], v196 offset:64000\n"
    "s_waitcnt lgkmcnt(9)\n"
    "v_mfma_f32_32x32x16_bf16 v[160:175], v[176:179], v[128:131], v[160:175]\n"
    "s_waitcnt lgkmcnt(6)\n"
    "v_mfma_f32_32x32x16_bf16 v[160:175], v[180:183], v[132:135], v[160:175]\n"
    "s_waitcnt lgkmcnt(3)\n"
    "v_mfma_f32_32x32x16_bf16 v[160:175], v[184:187], v[136:139], v[160:175]\n"
    "s_waitcnt lgkmcnt(0)\n"
    "v_mfma_f32_32x32x16_bf16 v[160:175], v[188:191], v[140:143], v[160:175]\n"
    "s_setprio 1\n"
    "v_mul_f32 v246, 0x3eb8aa3b, v246\n"
    "s_nop 9\n"
    "v_pk_mul_f32 v[160:161], v[160:161], v[246:247] op_sel_hi:[1,0]\n"
    "v_pk_mul_f32 v[162:163], v[162:163], v[246:247] op_sel_hi:[1,0]\n"
    "v_pk_mul_f32 v[164:165], v[164:165], v[246:247] op_sel_hi:[1,0]\n"
    "v_pk_mul_f32 v[166:167], v[166:167], v[246:247] op_sel_hi:[1,0]\n"
    "v_pk_mul_f32 v[168:169], v[168:169], v[246:247] op_sel_hi:[1,0]\n"
    "v_pk_mul_f32 v[170:171], v[170:171], v[246:247] op_sel_hi:[1,0]\n"
    "v_pk_mul_f32 v[172:173], v[172:173], v[246:247] op_sel_hi:[1,0]\n"
    "v_pk_mul_f32 v[174:175], v[174:175], v[246:247] op_sel_hi:[1,0]\n"
    "v_cvt_pk_bf16_f32 v128, v160, v161\n"
    "v_cvt_pk_bf16_f32 v129, v162, v163\n"
    "v_cvt_pk_bf16_f32 v130, v164, v165\n"
    "v_cvt_pk_bf16_f32 v131, v166, v167\n"
    "ds_write_b128 v198, v[128:131]\n"
    "v_cvt_pk_bf16_f32 v132, v168, v169\n"
    "v_cvt_pk_bf16_f32 v133, v170, v171\n"
    "v_cvt_pk_bf16_f32 v134, v172, v173\n"
    "v_cvt_pk_bf16_f32 v135, v174, v175\n"
    "ds_write_b128 v198, v[132:135] offset:2048\n"
    "s_waitcnt lgkmcnt(0)\n"
    "s_mov_b64 exec, 1\n"
    "ds_add_u32 v240, v241 offset:0\n"
    "s_mov_b64 exec, -1\n"
    "s_mov_b32 s71, 8388608\n"
    ".Lkg3078_spin:\n"
    "ds_read_b32 v246, v240 offset:36\n"
    "s_waitcnt lgkmcnt(0)\n"
    "v_cmp_gt_u32 vcc, 4, v246\n"
    "s_cbranch_vccz .Lkg3078_go\n"
    "s_sleep 1\n"
    "s_sub_u32 s71, s71, 1\n"
    "s_cbranch_scc0 .Lkg3078_spin\n"
    ".Lkg3078_go:\n"
    "s_mov_b32 s71, 8388608\n"
    ".Lqg3088_spin:\n"
    "ds_read_b32 v246, v240\n"
    "s_waitcnt lgkmcnt(0)\n"
    "v_cmp_gt_u32 vcc, 4, v246\n"
    "s_cbranch_vccz .Lqg3088_go\n"
    "s_sub_u32 s71, s71, 1\n"
    "s_cbranch_scc0 .Lqg3088_spin\n"
    ".Lqg3088_go:\n"
    "v_readfirstlane_b32 s56, v224\n"
    "v_readfirstlane_b32 s57, v225\n"
    "s_add_u32 m0, s70, 0\n"
    "s_nop 0\n"
    "buffer_load_dwordx4 v238, s[32:35], s68 offen lds\n"
    "buffer_load_dwordx4 v239, s[32:35], s68 offen offset:1024 lds\n"
    "buffer_load_dwordx4 v222, s[32:35], s68 offen offset:2048 lds\n"
    "buffer_load_dwordx4 v223, s[32:35], s68 offen offset:3072 lds\n"
    "s_add_u32 m0, s70, 4096\n"
    "s_nop 0\n"
    "buffer_load_dwordx4 v238, s[36:39], s68 offen lds\n"
    "buffer_load_dwordx4 v239, s[36:39], s68 offen offset:1024 lds\n"
    "buffer_load_dwordx4 v222, s[36:39], s68 offen offset:2048 lds\n"
    "buffer_load_dwordx4 v223, s[36:39], s68 offen offset:3072 lds\n"
    "s_add_u32 s68, s68, s74\n"
    "s_add_u32 m0, s70, 8192\n"
    "s_nop 0\n"
    "buffer_load_dwordx4 v238, s[32:35], s68 offen lds\n"
    "buffer_load_dwordx4 v239, s[32:35], s68 offen offset:1024 lds\n"
    "buffer_load_dwordx4 v222, s[32:35], s68 offen offset:2048 lds\n"
    "buffer_load_dwordx4 v223, s[32:35], s68 offen offset:3072 lds\n"
    "ds_read_b128 v[160:163], v228\n"
    "ds_read_b128 v[164:167], v228 offset:256\n"
    "ds_read_b128 v[168:171], v228 offset:32768\n"
    "ds_read_b128 v[172:175], v228 offset:33024\n"
    "s_setprio 0\n"
    "s_waitcnt vmcnt(8)\n"
    "ds_read_b64_tr_b16 v[128:129], v230\n"
    "ds_read_b64_tr_b16 v[130:131], v230 offset:512\n"
    "ds_read_b64_tr_b16 v[132:133], v231\n"
    "ds_read_b64_tr_b16 v[134:135], v231 offset:512\n"
    "ds_read_b64_tr_b16 v[136:137], v232\n"
    "ds_read_b64_tr_b16 v[138:139], v232 offset:512\n"
    "ds_read_b64_tr_b16 v[140:141], v233\n"
    "ds_read_b64_tr_b16 v[142:143], v233 offset:512\n"
    "s_add_u32 m0, s70, 12288\n"
    "s_nop 0\n"
    "buffer_load_dwordx4 v238, s[36:39], s68 offen lds\n"
    "buffer_load_dwordx4 v239, s[36:39], s68 offen offset:1024 lds\n"
    "buffer_load_dwordx4 v222, s[36:39], s68 offen offset:2048 lds\n"
    "buffer_load_dwordx4 v223, s[36:39], s68 offen offset:3072 lds\n"
    "s_add_u32 s68, s68, s74\n"
    "s_mov_b32 s71, 7\n"
    "s_waitcnt lgkmcnt(0)\n"
    "s_add_u32 m0, s70, 0\n"
    "s_nop 0\n"
    "buffer_load_dwordx4 v238, s[32:35], s68 offen lds\n"
    "buffer_load_dwordx4 v239, s[32:35], s68 offen offset:1024 lds\n"
    "buffer_load_dwordx4 v222, s[32:35], s68 offen offset:2048 lds\n"
    "buffer_load_dwordx4 v223, s[32:35], s68 offen offset:3072 lds\n"
    ".p2align 6\n"
    ".Lgf_loop:\n"
    "s_waitcnt vmcnt(12)\n"
    "v_mfma_f32_16x16x32_bf16 v[0:3], v[128:131], v[160:163], v[0:3]\n"
    "v_mfma_f32_16x16x32_bf16 v[4:7], v[128:131], v[164:167], v[4:7]\n"
    "ds_read_b64_tr_b16 v[144:145], v230 offset:4096\n"
    "ds_read_b64_tr_b16 v[146:147], v230 offset:4608\n"
    "ds_read_b64_tr_b16 v[148:149], v231 offset:4096\n"
    "v_mfma_f32_16x16x32_bf16 v[8:11], v[128:131], v[168:171], v[8:11]\n"
    "v_mfma_f32_16x16x32_bf16 v[12:15], v[128:131], v[172:175], v[12:15]\n"
    "ds_read_b64_tr_b16 v[150:151], v231 offset:4608\n"
    "ds_read_b64_tr_b16 v[152:153], v232 offset:4096\n"
    "ds_read_b64_tr_b16 v[154:155], v232 offset:4608\n"
    "v_mfma_f32_16x16x32_bf16 v[16:19], v[132:135], v[160:163], v[16:19]\n"
    "v_mfma_f32_16x16x32_bf16 v[20:23], v[132:135], v[164:167], v[20:23]\n"
    "ds_read_b64_tr_b16 v[156:157], v233 offset:4096\n"
    "ds_read_b64_tr_b16 v[158:159], v233 offset:4608\n"
    "v_mfma_f32_16x16x32_bf16 v[24:27], v[132:135], v[168:171], v[24:27]\n"
    "v_mfma_f32_16x16x32_bf16 v[28:31], v[132:135], v[172:175], v[28:31]\n"
    "ds_read_b128 v[176:179], v229 offset:2048\n"
    "ds_read_b128 v[180:183], v229 offset:2304\n"
    "v_mfma_f32_16x16x32_bf16 v[32:35], v[136:139], v[160:163], v[32:35]\n"
    "s_add_u32 m0, s70, 4096\n"
    "v_mfma_f32_16x16x32_bf16 v[36:39], v[136:139], v[164:167], v[36:39]\n"
    "s_waitcnt vmcnt(6) lgkmcnt(0)\n"
    "buffer_load_dwordx4 v238, s[36:39], s68 offen lds\n"
    "v_mfma_f32_16x16x32_bf16 v[40:43], v[136:139], v[168:171], v[40:43]\n"
    "v_mfma_f32_16x16x32_bf16 v[44:47], v[136:139], v[172:175], v[44:47]\n"
    "buffer_load_dwordx4 v239, s[36:39], s68 offen offset:1024 lds\n"
    "v_mfma_f32_16x16x32_bf16 v[48:51], v[140:143], v[160:163], v[48:51]\n"
    "v_mfma_f32_16x16x32_bf16 v[52:55], v[140:143], v[164:167], v[52:55]\n"
    "buffer_load_dwordx4 v222, s[36:39], s68 offen offset:2048 lds\n"
    "v_mfma_f32_16x16x32_bf16 v[56:59], v[140:143], v[168:171], v[56:59]\n"
    "v_mfma_f32_16x16x32_bf16 v[60:63], v[140:143], v[172:175], v[60:63]\n"
    "buffer_load_dwordx4 v223, s[36:39], s68 offen offset:3072 lds\n"
    "s_add_u32 s68, s68, s74\n"
    "s_waitcnt vmcnt(12)\n"
    "v_mfma_f32_16x16x32_bf16 v[64:67], v[144:147], v[160:163], v[64:67]\n"
    "v_mfma_f32_16x16x32_bf16 v[68:71], v[144:147], v[164:167], v[68:71]\n"
    "ds_read_b64_tr_b16 v[128:129], v230 offset:8192\n"
    "ds_read_b64_tr_b16 v[130:131], v230 offset:8704\n"
    "ds_read_b64_tr_b16 v[132:133], v231 offset:8192\n"
    "v_mfma_f32_16x16x32_bf16 v[72:75], v[144:147], v[168:171], v[72:75]\n"
    "v_mfma_f32_16x16x32_bf16 v[76:79], v[144:147], v[172:175], v[76:79]\n"
    "ds_read_b64_tr_b16 v[134:135], v231 offset:8704\n"
    "ds_read_b64_tr_b16 v[136:137], v232 offset:8192\n"
    "ds_read_b64_tr_b16 v[138:139], v232 offset:8704\n"
    "v_mfma_f32_16x16x32_bf16 v[80:83], v[148:151], v[160:163], v[80:83]\n"
    "v_mfma_f32_16x16x32_bf16 v[84:87], v[148:151], v[164:167], v[84:87]\n"
    "ds_read_b64_tr_b16 v[140:141], v233 offset:8192\n"
    "ds_read_b64_tr_b16 v[142:143], v233 offset:8704\n"
    "v_mfma_f32_16x16x32_bf16 v[88:91], v[148:151], v[168:171], v[88:91]\n"
    "v_mfma_f32_16x16x32_bf16 v[92:95], v[148:151], v[172:175], v[92:95]\n"
    "ds_read_b128 v[184:187], v229 offset:34816\n"
    "ds_read_b128 v[188:191], v229 offset:35072\n"
    "v_mfma_f32_16x16x32_bf16 v[96:99], v[152:155], v[160:163], v[96:99]\n"
    "s_add_u32 m0, s70, 8192\n"
    "v_mfma_f32_16x16x32_bf16 v[100:103], v[152:155], v[164:167], v[100:103]\n"
    "s_waitcnt vmcnt(6) lgkmcnt(0)\n"
    "buffer_load_dwordx4 v238, s[32:35], s68 offen lds\n"
    "v_mfma_f32_16x16x32_bf16 v[104:107], v[152:155], v[168:171], v[104:107]\n"
    "v_mfma_f32_16x16x32_bf16 v[108:111], v[152:155], v[172:175], v[108:111]\n"
    "buffer_load_dwordx4 v239, s[32:35], s68 offen offset:1024 lds\n"
    "v_mfma_f32_16x16x32_bf16 v[112:115], v[156:159], v[160:163], v[112:115]\n"
    "v_mfma_f32_16x16x32_bf16 v[116:119], v[156:159], v[164:167], v[116:119]\n"
    "buffer_load_dwordx4 v222, s[32:35], s68 offen offset:2048 lds\n"
    "v_mfma_f32_16x16x32_bf16 v[120:123], v[156:159], v[168:171], v[120:123]\n"
    "v_mfma_f32_16x16x32_bf16 v[124:127], v[156:159], v[172:175], v[124:127]\n"
    "buffer_load_dwordx4 v223, s[32:35], s68 offen offset:3072 lds\n"
    "s_waitcnt vmcnt(12)\n"
    "v_mfma_f32_16x16x32_bf16 v[0:3], v[128:131], v[176:179], v[0:3]\n"
    "v_mfma_f32_16x16x32_bf16 v[4:7], v[128:131], v[180:183], v[4:7]\n"
    "ds_read_b64_tr_b16 v[144:145], v230 offset:12288\n"
    "ds_read_b64_tr_b16 v[146:147], v230 offset:12800\n"
    "ds_read_b64_tr_b16 v[148:149], v231 offset:12288\n"
    "v_mfma_f32_16x16x32_bf16 v[8:11], v[128:131], v[184:187], v[8:11]\n"
    "v_mfma_f32_16x16x32_bf16 v[12:15], v[128:131], v[188:191], v[12:15]\n"
    "ds_read_b64_tr_b16 v[150:151], v231 offset:12800\n"
    "ds_read_b64_tr_b16 v[152:153], v232 offset:12288\n"
    "ds_read_b64_tr_b16 v[154:155], v232 offset:12800\n"
    "v_mfma_f32_16x16x32_bf16 v[16:19], v[132:135], v[176:179], v[16:19]\n"
    "v_mfma_f32_16x16x32_bf16 v[20:23], v[132:135], v[180:183], v[20:23]\n"
    "ds_read_b64_tr_b16 v[156:157], v233 offset:12288\n"
    "ds_read_b64_tr_b16 v[158:159], v233 offset:12800\n"
    "v_mfma_f32_16x16x32_bf16 v[24:27], v[132:135], v[184:187], v[24:27]\n"
    "v_mfma_f32_16x16x32_bf16 v[28:31], v[132:135], v[188:191], v[28:31]\n"
    "ds_read_b128 v[160:163], v228 offset:4096\n"
    "ds_read_b128 v[164:167], v228 offset:4352\n"
    "v_mfma_f32_16x16x32_bf16 v[32:35], v[136:139], v[176:179], v[32:35]\n"
    "s_add_u32 m0, s70, 12288\n"
    "v_mfma_f32_16x16x32_bf16 v[36:39], v[136:139], v[180:183], v[36:39]\n"
    "s_waitcnt vmcnt(6) lgkmcnt(0)\n"
    "buffer_load_dwordx4 v238, s[36:39], s68 offen lds\n"
    "v_mfma_f32_16x16x32_bf16 v[40:43], v[136:139], v[184:187], v[40:43]\n"
    "v_mfma_f32_16x16x32_bf16 v[44:47], v[136:139], v[188:191], v[44:47]\n"
    "buffer_load_dwordx4 v239, s[36:39], s68 offen offset:1024 lds\n"
    "v_mfma_f32_16x16x32_bf16 v[48:51], v[140:143], v[176:179], v[48:51]\n"
    "v_mfma_f32_16x16x32_bf16 v[52:55], v[140:143], v[180:183], v[52:55]\n"
    "buffer_load_dwordx4 v222, s[36:39], s68 offen offset:2048 lds\n"
    "v_mfma_f32_16x16x32_bf16 v[56:59], v[140:143], v[184:187], v[56:59]\n"
    "v_mfma_f32_16x16x32_bf16 v[60:63], v[140:143], v[188:191], v[60:63]\n"
    "buffer_load_dwordx4 v223, s[36:39], s68 offen offset:3072 lds\n"
    "s_add_u32 s68, s68, s74\n"
    "s_cmp_eq_u32 s71, 0\n"
    "s_cbranch_scc1 .Lgf_last\n"
    "s_cmp_lg_u32 s68, s69\n"
    "s_cbranch_scc1 .Lgf_nosw\n"
    "s_mov_b64 s[32:33], s[44:45]\n"
    "s_mov_b32 s34, s46\n"
    "s_add_u32 s36, s44, 32768\n"
    "s_addc_u32 s37, s45, 0\n"
    "s_sub_u32 s38, s46, 32768\n"
    "s_lshl_b32 s68, s75, 17\n"
    "s_add_u32 s69, s68, 0x20000\n"
    "s_mov_b32 s74, 65536\n"
    "s_setprio 1\n"
    "v_add_u32 v238, 0, v234\n"
    "v_add_u32 v239, 7168, v234\n"
    "v_add_u32 v222, 14336, v234\n"
    "v_add_u32 v223, 21504, v234\n"
    "s_setprio 0\n"
    ".Lgf_nosw:\n"
    "s_waitcnt vmcnt(12)\n"
    "v_mfma_f32_16x16x32_bf16 v[64:67], v[144:147], v[176:179], v[64:67]\n"
    "v_mfma_f32_16x16x32_bf16 v[68:71], v[144:147], v[180:183], v[68:71]\n"
    "ds_read_b64_tr_b16 v[128:129], v230\n"
    "ds_read_b64_tr_b16 v[130:131], v230 offset:512\n"
    "ds_read_b64_tr_b16 v[132:133], v231\n"
    "v_mfma_f32_16x16x32_bf16 v[72:75], v[144:147], v[184:187], v[72:75]\n"
    "v_mfma_f32_16x16x32_bf16 v[76:79], v[144:147], v[188:191], v[76:79]\n"
    "ds_read_b64_tr_b16 v[134:135], v231 offset:512\n"
    "ds_read_b64_tr_b16 v[136:137], v232\n"
    "ds_read_b64_tr_b16 v[138:139], v232 offset:512\n"
    "v_mfma_f32_16x16x32_bf16 v[80:83], v[148:151], v[176:179], v[80:83]\n"
    "v_mfma_f32_16x16x32_bf16 v[84:87], v[148:151], v[180:183], v[84:87]\n"
    "ds_read_b64_tr_b16 v[140:141], v233\n"
    "ds_read_b64_tr_b16 v[142:143], v233 offset:512\n"
    "v_mfma_f32_16x16x32_bf16 v[88:91], v[148:151], v[184:187], v[88:91]\n"
    "v_mfma_f32_16x16x32_bf16 v[92:95], v[148:151], v[188:191], v[92:95]\n"
    "ds_read_b128 v[168:171], v228 offset:36864\n"
    "ds_read_b128 v[172:175], v228 offset:37120\n"
    "v_mfma_f32_16x16x32_bf16 v[96:99], v[152:155], v[176:179], v[96:99]\n"
    "s_add_u32 m0, s70, 0\n"
    "v_mfma_f32_16x16x32_bf16 v[100:103], v[152:155], v[180:183], v[100:103]\n"
    "s_waitcnt vmcnt(6) lgkmcnt(0)\n"
    "s_setprio 1\n"
    "v_add_u32 v228, 4096, v228\n"
    "v_add_u32 v229, 4096, v229\n"
    "s_setprio 0\n"
    "buffer_load_dwordx4 v238, s[32:35], s68 offen lds\n"
    "v_mfma_f32_16x16x32_bf16 v[104:107], v[152:155], v[184:187], v[104:107]\n"
    "v_mfma_f32_16x16x32_bf16 v[108:111], v[152:155], v[188:191], v[108:111]\n"
    "buffer_load_dwordx4 v239, s[32:35], s68 offen offset:1024 lds\n"
    "v_mfma_f32_16x16x32_bf16 v[112:115], v[156:159], v[176:179], v[112:115]\n"
    "v_mfma_f32_16x16x32_bf16 v[116:119], v[156:159], v[180:183], v[116:119]\n"
    "buffer_load_dwordx4 v222, s[32:35], s68 offen offset:2048 lds\n"
    "v_mfma_f32_16x16x32_bf16 v[120:123], v[156:159], v[184:187], v[120:123]\n"
    "v_mfma_f32_16x16x32_bf16 v[124:127], v[156:159], v[188:191], v[124:127]\n"
    "buffer_load_dwordx4 v223, s[32:35], s68 offen offset:3072 lds\n"
    "s_cmp_lg_u32 s71, 3\n"
    "s_cbranch_scc1 .Lgf_nomark\n"
    "buffer_store_dwordx4 v[248:251], v242, s[28:31], s11 offen sc1\n"
    "buffer_store_dwordx4 v[248:251], v242, s[28:31], s11 offen offset:1024 sc1\n"
    ".Lgf_nomark:\n"
    "s_sub_u32 s71, s71, 1\n"
    "s_cbranch_scc0 .Lgf_loop\n"
    ".Lgf_last:\n"
    "s_setprio 1\n"
    "s_mul_i32 s58, s53, 14\n"
    "s_and_b32 s59, s53, 1\n"
    "s_lshl_b32 s59, s59, 1\n"
    "s_add_u32 s58, s58, s59\n"
    "s_lshl_b32 s59, s75, 2\n"
    "s_add_u32 s58, s58, s59\n"
    "s_lshl_b32 s58, s58, 11\n"
    "s_add_u32 s58, s58, s72\n"
    "s_add_u32 s59, s58, 4096\n"
    "v_mfma_f32_16x16x32_bf16 v[64:67], v[144:147], v[176:179], v[64:67]\n"
    "v_mfma_f32_16x16x32_bf16 v[68:71], v[144:147], v[180:183], v[68:71]\n"
    "v_max_f32 v0, 0, v0\n"
    "v_max_f32 v1, 0, v1\n"
    "v_max_f32 v2, 0, v2\n"
    "v_max_f32 v3, 0, v3\n"
    "v_max_f32 v4, 0, v4\n"
    "v_max_f32 v5, 0, v5\n"
    "v_max_f32 v6, 0, v6\n"
    "v_max_f32 v7, 0, v7\n"
    "v_max_f32 v16, 0, v16\n"
    "v_max_f32 v17, 0, v17\n"
    "v_max_f32 v18, 0, v18\n"
    "v_max_f32 v19, 0, v19\n"
    "v_max_f32 v20, 0, v20\n"
    "v_max_f32 v21, 0, v21\n"
    "v_max_f32 v22, 0, v22\n"
    "v_max_f32 v23, 0, v23\n"
    "v_max_f32 v8, 0, v8\n"
    "v_max_f32 v9, 0, v9\n"
    "v_mfma_f32_16x16x32_bf16 v[80:83], v[148:151], v[176:179], v[80:83]\n"
    "v_mfma_f32_16x16x32_bf16 v[84:87], v[148:151], v[180:183], v[84:87]\n"
    "v_max_f32 v10, 0, v10\n"
    "v_max_f32 v11, 0, v11\n"
    "v_max_f32 v12, 0, v12\n"
    "v_max_f32 v13, 0, v13\n"
    "v_max_f32 v14, 0, v14\n"
    "v_max_f32 v15, 0, v15\n"
    "v_max_f32 v24, 0, v24\n"
    "v_max_f32 v25, 0, v25\n"
    "v_max_f32 v26, 0, v26\n"
    "v_max_f32 v27, 0, v27\n"
    "v_max_f32 v28, 0, v28\n"
    "v_max_f32 v29, 0, v29\n"
    "v_max_f32 v30, 0, v30\n"
    "v_max_f32 v31, 0, v31\n"
    "v_max_f32 v32, 0, v32\n"
    "v_max_f32 v33, 0, v33\n"
    "v_max_f32 v34, 0, v34\n"
    "v_max_f32 v35, 0, v35\n"
    "v_mfma_f32_16x16x32_bf16 v[72:75], v[144:147], v[184:187], v[72:75]\n"
    "v_mfma_f32_16x16x32_bf16 v[76:79], v[144:147], v[188:191], v[76:79]\n"
    "v_max_f32 v36, 0, v36\n"
    "v_max_f32 v37, 0, v37\n"
    "v_max_f32 v38, 0, v38\n"
    "v_max_f32 v39, 0, v39\n"
    "v_max_f32 v48, 0, v48\n"
    "v_max_f32 v49, 0, v49\n"
    "v_max_f32 v50, 0, v50\n"
    "v_max_f32 v51, 0, v51\n"
    "v_max_f32 v52, 0, v52\n"
    "v_max_f32 v53, 0, v53\n"
    "v_max_f32 v54, 0, v54\n"
    "v_max_f32 v55, 0, v55\n"
    "v_max_f32 v40, 0, v40\n"
    "v_max_f32 v41, 0, v41\n"
    "v_max_f32 v42, 0, v42\n"
    "v_max_f32 v43, 0, v43\n"
    "v_max_f32 v44, 0, v44\n"
    "v_max_f32 v45, 0, v45\n"
    "v_mfma_f32_16x16x32_bf16 v[88:91], v[148:151], v[184:187], v[88:91]\n"
    "v_mfma_f32_16x16x32_bf16 v[92:95], v[148:151], v[188:191], v[92:95]\n"
    "v_max_f32 v46, 0, v46\n"
    "v_max_f32 v47, 0, v47\n"
    "v_max_f32 v56, 0, v56\n"
    "v_max_f32 v57, 0, v57\n"
    "v_max_f32 v58, 0, v58\n"
    "v_max_f32 v59, 0, v59\n"
    "v_max_f32 v60, 0, v60\n"
    "v_max_f32 v61, 0, v61\n"
    "v_max_f32 v62, 0, v62\n"
    "v_max_f32 v63, 0, v63\n"
    "v_pk_mul_f32 v[0:1], v[0:1], v[64:65]\n"
    "v_pk_mul_f32 v[2:3], v[2:3], v[66:67]\n"
    "v_pk_mul_f32 v[4:5], v[4:5], v[68:69]\n"
    "v_pk_mul_f32 v[6:7], v[6:7], v[70:71]\n"
    "v_pk_mul_f32 v[16:17], v[16:17], v[80:81]\n"
    "v_pk_mul_f32 v[18:19], v[18:19], v[82:83]\n"
    "v_pk_mul_f32 v[20:21], v[20:21], v[84:85]\n"
    "v_pk_mul_f32 v[22:23], v[22:23], v[86:87]\n"
    "v_mfma_f32_16x16x32_bf16 v[96:99], v[152:155], v[176:179], v[96:99]\n"
    "v_mfma_f32_16x16x32_bf16 v[100:103], v[152:155], v[180:183], v[100:103]\n"
    "v_cvt_pk_bf16_f32 v128, v0, v1\n"
    "v_cvt_pk_bf16_f32 v129, v2, v3\n"
    "v_cvt_pk_bf16_f32 v132, v4, v5\n"
    "v_cvt_pk_bf16_f32 v133, v6, v7\n"
    "v_cvt_pk_bf16_f32 v130, v16, v17\n"
    "v_cvt_pk_bf16_f32 v131, v18, v19\n"
    "v_cvt_pk_bf16_f32 v134, v20, v21\n"
    "v_cvt_pk_bf16_f32 v135, v22, v23\n"
    "v_permlane16_swap_b32 v128, v132\n"
    "v_permlane16_swap_b32 v129, v133\n"
    "v_permlane16_swap_b32 v130, v134\n"
    "v_permlane16_swap_b32 v131, v135\n"
    "buffer_store_dwordx4 v[128:131], v242, s[28:31], s58 offen sc1\n"
    "buffer_store_dwordx4 v[132:135], v242, s[28:31], s58 offen offset:2048 sc1\n"
    "v_pk_mul_f32 v[8:9], v[8:9], v[72:73]\n"
    "v_pk_mul_f32 v[10:11], v[10:11], v[74:75]\n"
    "v_pk_mul_f32 v[12:13], v[12:13], v[76:77]\n"
    "v_pk_mul_f32 v[14:15], v[14:15], v[78:79]\n"
    "v_mfma_f32_16x16x32_bf16 v[112:115], v[156:159], v[176:179], v[112:115]\n"
    "v_mfma_f32_16x16x32_bf16 v[116:119], v[156:159], v[180:183], v[116:119]\n"
    "v_pk_mul_f32 v[24:25], v[24:25], v[88:89]\n"
    "v_pk_mul_f32 v[26:27], v[26:27], v[90:91]\n"
    "v_pk_mul_f32 v[28:29], v[28:29], v[92:93]\n"
    "v_pk_mul_f32 v[30:31], v[30:31], v[94:95]\n"
    "v_cvt_pk_bf16_f32 v136, v8, v9\n"
    "v_cvt_pk_bf16_f32 v137, v10, v11\n"
    "v_cvt_pk_bf16_f32 v140, v12, v13\n"
    "v_cvt_pk_bf16_f32 v141, v14, v15\n"
    "v_cvt_pk_bf16_f32 v138, v24, v25\n"
    "v_cvt_pk_bf16_f32 v139, v26, v27\n"
    "v_cvt_pk_bf16_f32 v142, v28, v29\n"
    "v_cvt_pk_bf16_f32 v143, v30, v31\n"
    "v_permlane16_swap_b32 v136, v140\n"
    "v_permlane16_swap_b32 v137, v141\n"
    "v_permlane16_swap_b32 v138, v142\n"
    "v_permlane16_swap_b32 v139, v143\n"
    "buffer_store_dwordx4 v[136:139], v242, s[28:31], s58 offen offset:1024 sc1\n"
    "buffer_store_dwordx4 v[140:143], v242, s[28:31], s58 offen offset:3072 sc1\n"
    "v_mfma_f32_16x16x32_bf16 v[104:107], v[152:155], v[184:187], v[104:107]\n"
    "v_mfma_f32_16x16x32_bf16 v[108:111], v[152:155], v[188:191], v[108:111]\n"
    "v_mfma_f32_16x16x32_bf16 v[120:123], v[156:159], v[184:187], v[120:123]\n"
    "v_mfma_f32_16x16x32_bf16 v[124:127], v[156:159], v[188:191], v[124:127]\n"
    "v_pk_mul_f32 v[32:33], v[32:33], v[96:97]\n"
    "v_pk_mul_f32 v[34:35], v[34:35], v[98:99]\n"
    "v_pk_mul_f32 v[36:37], v[36:37], v[100:101]\n"
    "v_pk_mul_f32 v[38:39], v[38:39], v[102:103]\n"
    "v_pk_mul_f32 v[48:49], v[48:49], v[112:113]\n"
    "v_pk_mul_f32 v[50:51], v[50:51], v[114:115]\n"
    "v_pk_mul_f32 v[52:53], v[52:53], v[116:117]\n"
    "v_pk_mul_f32 v[54:55], v[54:55], v[118:119]\n"
    "v_cvt_pk_bf16_f32 v144, v32, v33\n"
    "v_cvt_pk_bf16_f32 v145, v34, v35\n"
    "v_cvt_pk_bf16_f32 v148, v36, v37\n"
    "v_cvt_pk_bf16_f32 v149, v38, v39\n"
    "v_cvt_pk_bf16_f32 v146, v48, v49\n"
    "v_cvt_pk_bf16_f32 v147, v50, v51\n"
    "v_cvt_pk_bf16_f32 v150, v52, v53\n"
    "v_cvt_pk_bf16_f32 v151, v54, v55\n"
    "v_permlane16_swap_b32 v144, v148\n"
    "v_permlane16_swap_b32 v145, v149\n"
    "v_permlane16_swap_b32 v146, v150\n"
    "v_permlane16_swap_b32 v147, v151\n"
    "buffer_store_dwordx4 v[144:147], v242, s[28:31], s59 offen sc1\n"
    "buffer_store_dwordx4 v[148:151], v242, s[28:31], s59 offen offset:2048 sc1\n"
    "v_pk_mul_f32 v[40:41], v[40:41], v[104:105]\n"
    "v_pk_mul_f32 v[42:43], v[42:43], v[106:107]\n"
    "v_pk_mul_f32 v[44:45], v[44:45], v[108:109]\n"
    "v_pk_mul_f32 v[46:47], v[46:47], v[110:111]\n"
    "v_pk_mul_f32 v[56:57], v[56:57], v[120:121]\n"
    "v_pk_mul_f32 v[58:59], v[58:59], v[122:123]\n"
    "v_pk_mul_f32 v[60:61], v[60:61], v[124:125]\n"
    "v_pk_mul_f32 v[62:63], v[62:63], v[126:127]\n"
    "v_cvt_pk_bf16_f32 v152, v40, v41\n"
    "v_cvt_pk_bf16_f32 v153, v42, v43\n"
    "v_cvt_pk_bf16_f32 v156, v44, v45\n"
    "v_cvt_pk_bf16_f32 v157, v46, v47\n"
    "v_cvt_pk_bf16_f32 v154, v56, v57\n"
    "v_cvt_pk_bf16_f32 v155, v58, v59\n"
    "v_cvt_pk_bf16_f32 v158, v60, v61\n"
    "v_cvt_pk_bf16_f32 v159, v62, v63\n"
    "v_permlane16_swap_b32 v152, v156\n"
    "v_permlane16_swap_b32 v153, v157\n"
    "v_permlane16_swap_b32 v154, v158\n"
    "v_permlane16_swap_b32 v155, v159\n"
    "buffer_store_dwordx4 v[152:155], v242, s[28:31], s59 offen offset:1024 sc1\n"
    "buffer_store_dwordx4 v[156:159], v242, s[28:31], s59 offen offset:3072 sc1\n"
    "v_add_u32 v232, s70, v245\n"
    "v_xor_b32 v233, 64, v232\n"
    "v_lshl_add_u32 v234, v237, 12, v234\n"
    "v_add_u32 v235, 15360, v234\n"
    "v_add_u32 v238, s76, v245\n"
    "v_xor_b32 v239, 64, v238\n"
    "s_waitcnt vmcnt(8)\n"
    "ds_read_b64_tr_b16 v[160:161], v232\n"
    "ds_read_b64_tr_b16 v[162:163], v232 offset:1024\n"
    "ds_read_b64_tr_b16 v[164:165], v233\n"
    "ds_read_b64_tr_b16 v[166:167], v233 offset:1024\n"
    "ds_read_b64_tr_b16 v[168:169], v232 offset:2048\n"
    "ds_read_b64_tr_b16 v[170:171], v232 offset:3072\n"
    "ds_read_b64_tr_b16 v[172:173], v233 offset:2048\n"
    "ds_read_b64_tr_b16 v[174:175], v233 offset:3072\n"
    "s_add_u32 m0, s70, 0\n"
    "s_add_u32 s68, s4, 0\n"
    "s_waitcnt vmcnt(63) lgkmcnt(4)\n"
    "buffer_load_dwordx4 v234, s[40:43], s68 offen lds\n"
    "buffer_load_dwordx4 v235, s[40:43], s68 offen offset:1024 lds\n"
    "ds_read_b64_tr_b16 v[176:177], v232 offset:4096\n"
    "ds_read_b64_tr_b16 v[178:179], v232 offset:5120\n"
    "ds_read_b64_tr_b16 v[180:181], v233 offset:4096\n"
    "ds_read_b64_tr_b16 v[182:183], v233 offset:5120\n"
    "s_add_u32 m0, s70, 2048\n"
    "s_add_u32 s68, s4, s62\n"
    "s_waitcnt vmcnt(63) lgkmcnt(4)\n"
    "buffer_load_dwordx4 v234, s[40:43], s68 offen lds\n"
    "buffer_load_dwordx4 v235, s[40:43], s68 offen offset:1024 lds\n"
    "ds_read_b64_tr_b16 v[184:185], v232 offset:6144\n"
    "ds_read_b64_tr_b16 v[186:187], v232 offset:7168\n"
    "ds_read_b64_tr_b16 v[188:189], v233 offset:6144\n"
    "ds_read_b64_tr_b16 v[190:191], v233 offset:7168\n"
    "s_add_u32 m0, s70, 4096\n"
    "s_add_u32 s68, s4, 32768\n"
    "s_waitcnt vmcnt(63) lgkmcnt(4)\n"
    "buffer_load_dwordx4 v234, s[40:43], s68 offen lds\n"
    "buffer_load_dwordx4 v235, s[40:43], s68 offen offset:1024 lds\n"
    "s_waitcnt vmcnt(6)\n"
    "s_mov_b64 exec, 1\n"
    "ds_add_rtn_u32 v246, v240, v241 offset:32\n"
    "ds_add_u32 v240, v241 offset:8\n"
    "s_waitcnt lgkmcnt(0)\n"
    "v_cmp_eq_u32 vcc, 2, v246\n"
    "s_mov_b64 exec, -1\n"
    "s_cbranch_vccz .Lgf_nopub\n"
    "s_add_u32 s64, s73, 32768\n"
    "s_mov_b64 exec, 3\n"
    "buffer_store_dwordx2 v[224:225], v227, s[28:31], s64 offen sc1\n"
    "s_mov_b64 exec, -1\n"
    ".Lgf_nopub:\n"
    "ds_read_b64_tr_b16 v[192:193], v232 offset:8192\n"
    "ds_read_b64_tr_b16 v[194:195], v232 offset:9216\n"
    "ds_read_b64_tr_b16 v[196:197], v233 offset:8192\n"
    "ds_read_b64_tr_b16 v[198:199], v233 offset:9216\n"
    "s_add_u32 m0, s70, 6144\n"
    "s_add_u32 s68, s5, 0\n"
    "s_waitcnt vmcnt(63) lgkmcnt(4)\n"
    "buffer_load_dwordx4 v234, s[40:43], s68 offen lds\n"
    "buffer_load_dwordx4 v235, s[40:43], s68 offen offset:1024 lds\n"
    "ds_read_b64_tr_b16 v[200:201], v232 offset:10240\n"
    "ds_read_b64_tr_b16 v[202:203], v232 offset:11264\n"
    "ds_read_b64_tr_b16 v[204:205], v233 offset:10240\n"
    "ds_read_b64_tr_b16 v[206:207], v233 offset:11264\n"
    "s_add_u32 m0, s70, 8192\n"
    "s_add_u32 s68, s5, s62\n"
    "s_waitcnt vmcnt(62) lgkmcnt(4)\n"
    "buffer_load_dwordx4 v234, s[40:43], s68 offen lds\n"
    "buffer_load_dwordx4 v235, s[40:43], s68 offen offset:1024 lds\n"
    "ds_read_b64_tr_b16 v[208:209], v232 offset:12288\n"
    "ds_read_b64_tr_b16 v[210:211], v232 offset:13312\n"
    "ds_read_b64_tr_b16 v[212:213], v233 offset:12288\n"
    "ds_read_b64_tr_b16 v[214:215], v233 offset:13312\n"
    "s_add_u32 m0, s70, 10240\n"
    "s_add_u32 s68, s5, 32768\n"
    "s_waitcnt vmcnt(62) lgkmcnt(4)\n"
    "buffer_load_dwordx4 v234, s[40:43], s68 offen lds\n"
    "buffer_load_dwordx4 v235, s[40:43], s68 offen offset:1024 lds\n"
    "ds_read_b64_tr_b16 v[216:217], v232 offset:14336\n"
    "ds_read_b64_tr_b16 v[218:219], v232 offset:15360\n"
    "ds_read_b64_tr_b16 v[220:221], v233 offset:14336\n"
    "ds_read_b64_tr_b16 v[222:223], v233 offset:15360\n"
    "s_add_u32 m0, s70, 12288\n"
    "s_add_u32 s68, s6, 0\n"
    "s_waitcnt vmcnt(62) lgkmcnt(4)\n"
    "buffer_load_dwordx4 v234, s[40:43], s68 offen lds\n"
    "buffer_load_dwordx4 v235, s[40:43], s68 offen offset:1024 lds\n"
    "s_add_u32 m0, s70, 14336\n"
    "s_add_u32 s68, s6, s62\n"
    "s_waitcnt vmcnt(62) lgkmcnt(0)\n"
    "buffer_load_dwordx4 v234, s[40:43], s68 offen lds\n"
    "buffer_load_dwordx4 v235, s[40:43], s68 offen offset:1024 lds\n"
    "s_mov_b32 s71, 8388608\n"
    ".Lex3584_spin:\n"
    "ds_read_b32 v246, v240 offset:24\n"
    "s_waitcnt lgkmcnt(0)\n"
    "v_cmp_gt_u32 vcc, 1, v246\n"
    "s_cbranch_vccz .Lex3584_go\n"
    "s_sleep 2\n"
    "s_sub_u32 s71, s71, 1\n"
    "s_cbranch_scc0 .Lex3584_spin\n"
    ".Lex3584_go:\n"
    "s_mov_b32 s77, 0\n"
    "s_setprio 0\n"
    "ds_read_b64_tr_b16 v[96:97], v238\n"
    "ds_read_b64_tr_b16 v[98:99], v238 offset:1024\n"
    "ds_read_b64_tr_b16 v[100:101], v239\n"
    "ds_read_b64_tr_b16 v[102:103], v239 offset:1024\n"
    "ds_read_b64_tr_b16 v[104:105], v238 offset:2048\n"
    "ds_read_b64_tr_b16 v[106:107], v238 offset:3072\n"
    "ds_read_b64_tr_b16 v[108:109], v239 offset:2048\n"
    "ds_read_b64_tr_b16 v[110:111], v239 offset:3072\n"
    "ds_read_b64_tr_b16 v[112:113], v238 offset:4096\n"
    "ds_read_b64_tr_b16 v[114:115], v238 offset:5120\n"
    "ds_read_b64_tr_b16 v[116:117], v239 offset:4096\n"
    "ds_read_b64_tr_b16 v[118:119], v239 offset:5120\n"
    "s_waitcnt lgkmcnt(8)\n"
    "ds_read_b64_tr_b16 v[120:121], v238 offset:6144\n"
    "ds_read_b64_tr_b16 v[122:123], v238 offset:7168\n"
    "ds_read_b64_tr_b16 v[124:125], v239 offset:6144\n"
    "ds_read_b64_tr_b16 v[126:127], v239 offset:7168\n"
    "v_mfma_f32_32x32x16_bf16 v[0:15], v[96:99], v[128:131], 0\n"
    "s_add_u32 m0, s76, 0\n"
    "s_add_u32 s68, s6, 32768\n"
    "buffer_load_dwordx4 v234, s[40:43], s68 offen lds\n"
    "v_mfma_f32_32x32x16_bf16 v[32:47], v[100:103], v[128:131], 0\n"
    "buffer_load_dwordx4 v235, s[40:43], s68 offen offset:1024 lds\n"
    "v_mfma_f32_32x32x16_bf16 v[16:31], v[96:99], v[136:139], 0\n"
    "v_mfma_f32_32x32x16_bf16 v[48:63], v[100:103], v[136:139], 0\n"
    "s_waitcnt lgkmcnt(8)\n"
    "v_mfma_f32_32x32x16_bf16 v[0:15], v[104:107], v[132:135], v[0:15]\n"
    "s_add_u32 m0, s76, 2048\n"
    "s_add_u32 s68, s7, 0\n"
    "buffer_load_dwordx4 v234, s[40:43], s68 offen lds\n"
    "v_mfma_f32_32x32x16_bf16 v[32:47], v[108:111], v[132:135], v[32:47]\n"
    "buffer_load_dwordx4 v235, s[40:43], s68 offen offset:1024 lds\n"
    "v_mfma_f32_32x32x16_bf16 v[16:31], v[104:107], v[140:143], v[16:31]\n"
    "v_mfma_f32_32x32x16_bf16 v[48:63], v[108:111], v[140:143], v[48:63]\n"
    "s_waitcnt lgkmcnt(4)\n"
    "v_mfma_f32_32x32x16_bf16 v[0:15], v[112:115], v[144:147], v[0:15]\n"
    "s_add_u32 m0, s76, 4096\n"
    "s_add_u32 s68, s7, s62\n"
    "buffer_load_dwordx4 v234, s[40:43], s68 offen lds\n"
    "v_mfma_f32_32x32x16_bf16 v[32:47], v[116:119], v[144:147], v[32:47]\n"
    "buffer_load_dwordx4 v235, s[40:43], s68 offen offset:1024 lds\n"
    "v_mfma_f32_32x32x16_bf16 v[16:31], v[112:115], v[152:155], v[16:31]\n"
    "v_mfma_f32_32x32x16_bf16 v[48:63], v[116:119], v[152:155], v[48:63]\n"
    "s_waitcnt lgkmcnt(0)\n"
    "v_mfma_f32_32x32x16_bf16 v[0:15], v[120:123], v[148:151], v[0:15]\n"
    "v_mfma_f32_32x32x16_bf16 v[32:47], v[124:127], v[148:151], v[32:47]\n"
    "v_mfma_f32_32x32x16_bf16 v[16:31], v[120:123], v[156:159], v[16:31]\n"
    "v_mfma_f32_32x32x16_bf16 v[48:63], v[124:127], v[156:159], v[48:63]\n"
    "s_setprio 1\n"
    "s_branch .Lg2\n"
    ".Lg3:\n"
    "s_mov_b32 s36, s16\n"
    "s_and_b32 s37, s17, 0xffff\n"
    "s_mov_b32 s38, 1835008\n"
    "s_mov_b32 s39, 0x20000\n"
    "s_bitcmp1_b32 s53, 0\n"
    "s_cselect_b32 s59, 0, 384\n"
    "s_add_u32 s58, s58, s59\n"
    "v_lshrrev_b32 v6, 2, v243\n"
    "v_mul_u32_u24 v6, 3584, v6\n"
    "v_lshlrev_b32 v14, 1, v237\n"
    "v_xor_b32 v14, v14, v8\n"
    "v_lshl_add_u32 v6, v14, 4, v6\n"
    "v_add_u32 v238, s58, v6\n"
    "v_add_u32 v239, 56320, v238\n"
    "v_lshl_add_u32 v6, v12, 3, v7\n"
    "v_lshl_add_u32 v6, v6, 6, v9\n"
    "v_add_u32 v230, s70, v6\n"
    "v_xor_b32 v231, 32, v230\n"
    "s_lshr_b32 s64, s75, 1\n"
    "s_lshl_b32 s65, s64, 6\n"
    "s_add_u32 s65, s65, 65536\n"
    "v_xor_b32 v68, s65, v11\n"
    "s_and_b32 s65, s75, 1\n"
    "s_lshl_b32 s67, s65, 15\n"
    "v_add_u32 v64, s67, v16\n"
    "v_add_u32 v65, s67, v17\n"
    "v_add_u32 v66, s67, v18\n"
    "v_add_u32 v67, s67, v19\n"
    "s_lshl_b32 s67, s65, 7\n"
    "s_add_u32 s67, s67, 161792\n"
    "v_lshl_add_u32 v69, v236, 2, s67\n"
    "s_lshl_b32 s65, s65, 10\n"
    "s_lshl_b32 s64, s64, 12\n"
    "s_add_u32 s65, s65, s64\n"
    "s_add_u32 s65, s65, 147456\n"
    "v_add_u32 v70, s65, v242\n"
    "v_lshrrev_b32 v6, 4, v243\n"
    "v_and_b32 v13, 15, v243\n"
    "v_lshlrev_b32 v14, 3, v243\n"
    "v_lshl_add_u32 v14, v6, 7, v14\n"
    "v_add_u32 v80, 131072, v14\n"
    "s_lshr_b32 s58, s53, 1\n"
    "s_and_b32 s58, s58, 3\n"
    "s_and_b32 s60, s58, 1\n"
    "s_lshl_b32 s59, s60, 8\n"
    "s_lshr_b32 s61, s58, 1\n"
    "s_lshl_b32 s62, s61, 15\n"
    "s_add_u32 s59, s59, s62\n"
    "v_xor_b32 v14, v13, v6\n"
    "v_lshlrev_b32 v14, 4, v14\n"
    "v_lshl_add_u32 v14, v6, 9, v14\n"
    "v_add_u32 v76, s59, v14\n"
    "v_xor_b32 v77, 64, v76\n"
    "s_lshl_b32 s62, s52, 1\n"
    "s_add_u32 s61, s61, s62\n"
    "s_lshl_b32 s61, s61, 11\n"
    "s_add_u32 s61, s61, 262144\n"
    "s_lshl_b32 s58, s58, 6\n"
    "s_add_u32 s58, s58, 161792\n"
    "s_lshl_b32 s63, s52, 3\n"
    "s_add_u32 s63, s63, s53\n"
    "s_lshl_b32 s63, s63, 3\n"
    "s_add_u32 s63, s63, 4096\n"
    "v_and_b32 v14, 1, v6\n"
    "v_lshrrev_b32 v15, 1, v6\n"
    "v_lshlrev_b32 v78, 4, v13\n"
    "v_lshlrev_b32 v81, 11, v243\n"
    "s_bitcmp1_b32 s53, 0\n"
    "s_cbranch_scc1 .Lg3_va\n"
    "v_lshl_add_u32 v78, v14, 9, v78\n"
    "v_lshl_add_u32 v78, v15, 3, v78\n"
    "s_lshl_b32 s60, s60, 8\n"
    "v_lshl_add_u32 v79, v13, 2, s58\n"
    "s_branch .Lg3_vb\n"
    ".Lg3_va:\n"
    "v_lshl_add_u32 v78, v15, 8, v78\n"
    "v_lshl_add_u32 v78, v14, 3, v78\n"
    "s_lshl_b32 s60, s60, 9\n"
    "s_add_u32 s60, s60, 1024\n"
    "v_lshl_add_u32 v79, v6, 4, s58\n"
    ".Lg3_vb:\n"
    "s_add_u32 s61, s61, s60\n"
    "s_waitcnt vmcnt(1)\n"
    "ds_read_b64_tr_b16 v[160:161], v80\n"
    "ds_read_b64_tr_b16 v[162:163], v80 offset:128\n"
    "ds_read_b64_tr_b16 v[164:165], v80 offset:1024\n"
    "ds_read_b64_tr_b16 v[166:167], v80 offset:1152\n"
    "ds_read_b64_tr_b16 v[168:169], v80 offset:2048\n"
    "ds_read_b64_tr_b16 v[170:171], v80 offset:2176\n"
    "ds_read_b64_tr_b16 v[172:173], v80 offset:3072\n"
    "ds_read_b64_tr_b16 v[174:175], v80 offset:3200\n"
    "s_waitcnt lgkmcnt(6)\n"
    "ds_read_b64_tr_b16 v[176:177], v80 offset:4096\n"
    "ds_read_b64_tr_b16 v[178:179], v80 offset:4224\n"
    "ds_read_b64_tr_b16 v[180:181], v80 offset:5120\n"
    "ds_read_b64_tr_b16 v[182:183], v80 offset:5248\n"
    "ds_read_b64_tr_b16 v[184:185], v80 offset:6144\n"
    "ds_read_b64_tr_b16 v[186:187], v80 offset:6272\n"
    "ds_read_b64_tr_b16 v[188:189], v80 offset:7168\n"
    "ds_read_b64_tr_b16 v[190:191], v80 offset:7296\n"
    "s_waitcnt lgkmcnt(6)\n"
    "ds_read_b64_tr_b16 v[192:193], v80 offset:8192\n"
    "ds_read_b64_tr_b16 v[194:195], v80 offset:8320\n"
    "ds_read_b64_tr_b16 v[196:197], v80 offset:9216\n"
    "ds_read_b64_tr_b16 v[198:199], v80 offset:9344\n"
    "ds_read_b64_tr_b16 v[200:201], v80 offset:10240\n"
    "ds_read_b64_tr_b16 v[202:203], v80 offset:10368\n"
    "ds_read_b64_tr_b16 v[204:205], v80 offset:11264\n"
    "ds_read_b64_tr_b16 v[206:207], v80 offset:11392\n"
    "s_waitcnt lgkmcnt(6)\n"
    "ds_read_b64_tr_b16 v[208:209], v80 offset:12288\n"
    "ds_read_b64_tr_b16 v[210:211], v80 offset:12416\n"
    "ds_read_b64_tr_b16 v[212:213], v80 offset:13312\n"
    "ds_read_b64_tr_b16 v[214:215], v80 offset:13440\n"
    "ds_read_b64_tr_b16 v[216:217], v80 offset:14336\n"
    "ds_read_b64_tr_b16 v[218:219], v80 offset:14464\n"
    "ds_read_b64_tr_b16 v[220:221], v80 offset:15360\n"
    "ds_read_b64_tr_b16 v[222:223], v80 offset:15488\n"
    "s_waitcnt lgkmcnt(6)\n"
    "s_waitcnt vmcnt(0)\n"
    "v_readfirstlane_b32 s56, v224\n"
    "v_readfirstlane_b32 s57, v225\n"
    "s_mul_i32 s56, s56, 0x9e3779b1\n"
    "s_add_u32 s56, s56, 0x7f4a7c15\n"
    "s_or_b32 s56, s56, 1\n"
    "s_mul_i32 s57, s57, 0x85ebca6d\n"
    "s_add_u32 s57, s57, 0xc2b2ae35\n"
    "v_mov_b32 v82, s56\n"
    "v_mov_b32 v83, s57\n"
    "ds_write_b64 v240, v[82:83] offset:48\n"
    "s_setprio 3\n"
    "s_mov_b32 s71, 8388608\n"
    ".Lb03778_spin:\n"
    "ds_read_b32 v246, v240 offset:28\n"
    "s_waitcnt lgkmcnt(0)\n"
    "v_cmp_gt_u32 vcc, 4, v246\n"
    "s_cbranch_vccz .Lb03778_go\n"
    "s_sub_u32 s71, s71, 1\n"
    "s_cbranch_scc0 .Lb03778_spin\n"
    ".Lb03778_go:\n"
    "s_bitcmp1_b32 s53, 0\n"
    "s_cbranch_scc1 .Lg3_vjob\n"
    "ds_read_b128 v[128:131], v76\n"
    "ds_read_b128 v[132:135], v77 offset:2048\n"
    "ds_read_b128 v[136:139], v76 offset:4096\n"
    "ds_read_b128 v[140:143], v77 offset:6144\n"
    "ds_read_b128 v[144:147], v76 offset:8192\n"
    "ds_read_b128 v[148:151], v77 offset:10240\n"
    "ds_read_b128 v[152:155], v76 offset:12288\n"
    "ds_read_b128 v[156:159], v77 offset:14336\n"
    "s_waitcnt lgkmcnt(7)\n"
    "v_mfma_f32_16x16x32_bf16 v[72:75], v[160:163], v[128:131], 0\n"
    "ds_read_b128 v[128:131], v76 offset:16384\n"
    "s_waitcnt lgkmcnt(7)\n"
    "v_mfma_f32_16x16x32_bf16 v[72:75], v[164:167], v[132:135], v[72:75]\n"
    "ds_read_b128 v[132:135], v77 offset:18432\n"
    "s_waitcnt lgkmcnt(7)\n"
    "v_mfma_f32_16x16x32_bf16 v[72:75], v[168:171], v[136:139], v[72:75]\n"
    "ds_read_b128 v[136:139], v76 offset:20480\n"
    "s_waitcnt lgkmcnt(7)\n"
    "v_mfma_f32_16x16x32_bf16 v[72:75], v[172:175], v[140:143], v[72:75]\n"
    "ds_read_b128 v[140:143], v77 offset:22528\n"
    "s_waitcnt lgkmcnt(7)\n"
    "v_mfma_f32_16x16x32_bf16 v[72:75], v[176:179], v[144:147], v[72:75]\n"
    "ds_read_b128 v[144:147], v76 offset:24576\n"
    "s_waitcnt lgkmcnt(7)\n"
    "v_mfma_f32_16x16x32_bf16 v[72:75], v[180:183], v[148:151], v[72:75]\n"
    "ds_read_b128 v[148:151], v77 offset:26624\n"
    "s_waitcnt lgkmcnt(7)\n"
    "v_mfma_f32_16x16x32_bf16 v[72:75], v[184:187], v[152:155], v[72:75]\n"
    "ds_read_b128 v[152:155], v76 offset:28672\n"
    "s_waitcnt lgkmcnt(7)\n"
    "v_mfma_f32_16x16x32_bf16 v[72:75], v[188:191], v[156:159], v[72:75]\n"
    "ds_read_b128 v[156:159], v77 offset:30720\n"
    "s_waitcnt lgkmcnt(7)\n"
    "v_mfma_f32_16x16x32_bf16 v[72:75], v[192:195], v[128:131], v[72:75]\n"
    "ds_read_b32 v88, v79\n"
    "s_waitcnt lgkmcnt(7)\n"
    "v_mfma_f32_16x16x32_bf16 v[72:75], v[196:199], v[132:135], v[72:75]\n"
    "s_waitcnt lgkmcnt(6)\n"
    "v_mfma_f32_16x16x32_bf16 v[72:75], v[200:203], v[136:139], v[72:75]\n"
    "s_waitcnt lgkmcnt(5)\n"
    "v_mfma_f32_16x16x32_bf16 v[72:75], v[204:207], v[140:143], v[72:75]\n"
    "s_waitcnt lgkmcnt(4)\n"
    "v_mfma_f32_16x16x32_bf16 v[72:75], v[208:211], v[144:147], v[72:75]\n"
    "s_waitcnt lgkmcnt(3)\n"
    "v_mfma_f32_16x16x32_bf16 v[72:75], v[212:215], v[148:151], v[72:75]\n"
    "s_waitcnt lgkmcnt(2)\n"
    "v_mfma_f32_16x16x32_bf16 v[72:75], v[216:219], v[152:155], v[72:75]\n"
    "s_waitcnt lgkmcnt(1)\n"
    "v_mfma_f32_16x16x32_bf16 v[72:75], v[220:223], v[156:159], v[72:75]\n"
    "s_waitcnt lgkmcnt(0)\n"
    "s_nop 6\n"
    "v_mul_f32 v72, v88, v72\n"
    "v_mul_f32 v73, v88, v73\n"
    "v_mul_f32 v74, v88, v74\n"
    "v_mul_f32 v75, v88, v75\n"
    "v_cvt_pk_bf16_f32 v84, v72, v73\n"
    "v_cvt_pk_bf16_f32 v85, v74, v75\n"
    "buffer_store_dwordx2 v[84:85], v78, s[28:31], s61 offen sc1\n"
    "s_branch .Lg3_pub\n"
    ".Lg3_vjob:\n"
    "ds_read_b128 v[128:131], v76\n"
    "ds_read_b128 v[132:135], v77 offset:2048\n"
    "ds_read_b128 v[136:139], v76 offset:4096\n"
    "ds_read_b128 v[140:143], v77 offset:6144\n"
    "ds_read_b128 v[144:147], v76 offset:8192\n"
    "ds_read_b128 v[148:151], v77 offset:10240\n"
    "ds_read_b128 v[152:155], v76 offset:12288\n"
    "ds_read_b128 v[156:159], v77 offset:14336\n"
    "s_waitcnt lgkmcnt(7)\n"
    "v_mfma_f32_16x16x32_bf16 v[72:75], v[128:131], v[160:163], 0\n"
    "ds_read_b128 v[128:131], v76 offset:16384\n"
    "s_waitcnt lgkmcnt(7)\n"
    "v_mfma_f32_16x16x32_bf16 v[72:75], v[132:135], v[164:167], v[72:75]\n"
    "ds_read_b128 v[132:135], v77 offset:18432\n"
    "s_waitcnt lgkmcnt(7)\n"
    "v_mfma_f32_16x16x32_bf16 v[72:75], v[136:139], v[168:171], v[72:75]\n"
    "ds_read_b128 v[136:139], v76 offset:20480\n"
    "s_waitcnt lgkmcnt(7)\n"
    "v_mfma_f32_16x16x32_bf16 v[72:75], v[140:143], v[172:175], v[72:75]\n"
    "ds_read_b128 v[140:143], v77 offset:22528\n"
    "s_waitcnt lgkmcnt(7)\n"
    "v_mfma_f32_16x16x32_bf16 v[72:75], v[144:147], v[176:179], v[72:75]\n"
    "ds_read_b128 v[144:147], v76 offset:24576\n"
    "s_waitcnt lgkmcnt(7)\n"
    "v_mfma_f32_16x16x32_bf16 v[72:75], v[148:151], v[180:183], v[72:75]\n"
    "ds_read_b128 v[148:151], v77 offset:26624\n"
    "s_waitcnt lgkmcnt(7)\n"
    "v_mfma_f32_16x16x32_bf16 v[72:75], v[152:155], v[184:187], v[72:75]\n"
    "ds_read_b128 v[152:155], v76 offset:28672\n"
    "s_waitcnt lgkmcnt(7)\n"
    "v_mfma_f32_16x16x32_bf16 v[72:75], v[156:159], v[188:191], v[72:75]\n"
    "ds_read_b128 v[156:159], v77 offset:30720\n"
    "s_waitcnt lgkmcnt(7)\n"
    "v_mfma_f32_16x16x32_bf16 v[72:75], v[128:131], v[192:195], v[72:75]\n"
    "ds_read_b128 v[88:91], v79\n"
    "s_waitcnt lgkmcnt(7)\n"
    "v_mfma_f32_16x16x32_bf16 v[72:75], v[132:135], v[196:199], v[72:75]\n"
    "s_waitcnt lgkmcnt(6)\n"
    "v_mfma_f32_16x16x32_bf16 v[72:75], v[136:139], v[200:203], v[72:75]\n"
    "s_waitcnt lgkmcnt(5)\n"
    "v_mfma_f32_16x16x32_bf16 v[72:75], v[140:143], v[204:207], v[72:75]\n"
    "s_waitcnt lgkmcnt(4)\n"
    "v_mfma_f32_16x16x32_bf16 v[72:75], v[144:147], v[208:211], v[72:75]\n"
    "s_waitcnt lgkmcnt(3)\n"
    "v_mfma_f32_16x16x32_bf16 v[72:75], v[148:151], v[212:215], v[72:75]\n"
    "s_waitcnt lgkmcnt(2)\n"
    "v_mfma_f32_16x16x32_bf16 v[72:75], v[152:155], v[216:219], v[72:75]\n"
    "s_waitcnt lgkmcnt(1)\n"
    "v_mfma_f32_16x16x32_bf16 v[72:75], v[156:159], v[220:223], v[72:75]\n"
    "s_waitcnt lgkmcnt(0)\n"
    "s_nop 6\n"
    "v_mul_f32 v72, v88, v72\n"
    "v_mul_f32 v73, v89, v73\n"
    "v_mul_f32 v74, v90, v74\n"
    "v_mul_f32 v75, v91, v75\n"
    "v_cvt_pk_bf16_f32 v84, v72, v73\n"
    "v_cvt_pk_bf16_f32 v85, v74, v75\n"
    "buffer_store_dwordx2 v[84:85], v78, s[28:31], s61 offen sc1\n"
    "v_max3_f32 v86, abs(v72), abs(v73), abs(v74)\n"
    "v_max_f32 v86, abs(v75), v86\n"
    "v_cmp_lt_f32 vcc, 0x53800000, v86\n"
    "s_nop 0\n"
    "s_cmp_lg_u64 vcc, 0\n"
    "s_cselect_b32 s64, 1, 0\n"
    "v_xor_b32 v82, s64, v82\n"
    ".Lg3_pub:\n"
    "s_waitcnt vmcnt(0)\n"
    "s_mov_b64 exec, 255\n"
    "buffer_store_dwordx2 v[82:83], v81, s[28:31], s63 offen sc1\n"
    "s_mov_b64 exec, -1\n"
    "s_setprio 0\n"
    "v_mov_b64 v[0:1], 0\n"
    "v_mov_b64 v[2:3], 0\n"
    "v_mov_b64 v[4:5], 0\n"
    "v_mov_b64 v[6:7], 0\n"
    "v_mov_b64 v[8:9], 0\n"
    "v_mov_b64 v[10:11], 0\n"
    "v_mov_b64 v[12:13], 0\n"
    "v_mov_b64 v[14:15], 0\n"
    "v_mov_b64 v[16:17], 0\n"
    "v_mov_b64 v[18:19], 0\n"
    "v_mov_b64 v[20:21], 0\n"
    "v_mov_b64 v[22:23], 0\n"
    "v_mov_b64 v[24:25], 0\n"
    "v_mov_b64 v[26:27], 0\n"
    "v_mov_b64 v[28:29], 0\n"
    "v_mov_b64 v[30:31], 0\n"
    "v_mov_b64 v[32:33], 0\n"
    "v_mov_b64 v[34:35], 0\n"
    "v_mov_b64 v[36:37], 0\n"
    "v_mov_b64 v[38:39], 0\n"
    "v_mov_b64 v[40:41], 0\n"
    "v_mov_b64 v[42:43], 0\n"
    "v_mov_b64 v[44:45], 0\n"
    "v_mov_b64 v[46:47], 0\n"
    "v_mov_b64 v[48:49], 0\n"
    "v_mov_b64 v[50:51], 0\n"
    "v_mov_b64 v[52:53], 0\n"
    "v_mov_b64 v[54:55], 0\n"
    "v_mov_b64 v[56:57], 0\n"
    "v_mov_b64 v[58:59], 0\n"
    "v_mov_b64 v[60:61], 0\n"
    "v_mov_b64 v[62:63], 0\n"
    "v_lshlrev_b32 v227, 7, v243\n"
    "v_and_b32 v226, 7, v243\n"
    "v_max_u32 v226, 1, v226\n"
    "v_xor_b32 v226, s53, v226\n"
    "v_lshlrev_b32 v226, 3, v226\n"
    "v_mov_b64 v[248:249], -1\n"
    "v_mov_b64 v[250:251], -1\n"
    "v_mov_b32 v224, s56\n"
    "v_mov_b32 v225, s57\n"
    "s_barrier\n"
    "buffer_load_dwordx4 v[252:255], v244, s[24:27], 0 offen\n"
    "ds_read_b32 v246, v69\n"
    "ds_read_b128 v[128:131], v64\n"
    "ds_read_b64_tr_b16 v[176:177], v68\n"
    "ds_read_b64_tr_b16 v[178:179], v68 offset:512\n"
    "ds_read_b128 v[132:135], v65 offset:1024\n"
    "ds_read_b64_tr_b16 v[180:181], v68 offset:2048\n"
    "ds_read_b64_tr_b16 v[182:183], v68 offset:2560\n"
    "ds_read_b128 v[136:139], v66 offset:2048\n"
    "ds_read_b64_tr_b16 v[184:185], v68 offset:4096\n"
    "ds_read_b64_tr_b16 v[186:187], v68 offset:4608\n"
    "ds_read_b128 v[140:143], v67 offset:3072\n"
    "ds_read_b64_tr_b16 v[188:189], v68 offset:6144\n"
    "ds_read_b64_tr_b16 v[190:191], v68 offset:6656\n"
    "s_waitcnt lgkmcnt(9)\n"
    "v_mfma_f32_32x32x16_bf16 v[160:175], v[176:179], v[128:131], 0\n"
    "ds_read_b128 v[128:131], v64 offset:4096\n"
    "ds_read_b64_tr_b16 v[176:177], v68 offset:8192\n"
    "ds_read_b64_tr_b16 v[178:179], v68 offset:8704\n"
    "s_waitcnt lgkmcnt(9)\n"
    "v_mfma_f32_32x32x16_bf16 v[160:175], v[180:183], v[132:135], v[160:175]\n"
    "ds_read_b128 v[132:135], v65 offset:5120\n"
    "ds_read_b64_tr_b16 v[180:181], v68 offset:10240\n"
    "ds_read_b64_tr_b16 v[182:183], v68 offset:10752\n"
    "s_waitcnt lgkmcnt(9)\n"
    "v_mfma_f32_32x32x16_bf16 v[160:175], v[184:187], v[136:139], v[160:175]\n"
    "ds_read_b128 v[136:139], v66 offset:6144\n"
    "ds_read_b64_tr_b16 v[184:185], v68 offset:12288\n"
    "ds_read_b64_tr_b16 v[186:187], v68 offset:12800\n"
    "s_waitcnt lgkmcnt(9)\n"
    "v_mfma_f32_32x32x16_bf16 v[160:175], v[188:191], v[140:143], v[160:175]\n"
    "ds_read_b128 v[140:143], v67 offset:7168\n"
    "ds_read_b64_tr_b16 v[188:189], v68 offset:14336\n"
    "ds_read_b64_tr_b16 v[190:191], v68 offset:14848\n"
    "s_waitcnt lgkmcnt(9)\n"
    "v_mfma_f32_32x32x16_bf16 v[160:175], v[176:179], v[128:131], v[160:175]\n"
    "ds_read_b128 v[128:131], v64 offset:8192\n"
    "ds_read_b64_tr_b16 v[176:177], v68 offset:16384\n"
    "ds_read_b64_tr_b16 v[178:179], v68 offset:16896\n"
    "s_waitcnt lgkmcnt(9)\n"
    "v_mfma_f32_32x32x16_bf16 v[160:175], v[180:183], v[132:135], v[160:175]\n"
    "ds_read_b128 v[132:135], v65 offset:9216\n"
    "ds_read_b64_tr_b16 v[180:181], v68 offset:18432\n"
    "ds_read_b64_tr_b16 v[182:183], v68 offset:18944\n"
    "s_waitcnt lgkmcnt(9)\n"
    "v_mfma_f32_32x32x16_bf16 v[160:175], v[184:187], v[136:139], v[160:175]\n"
    "ds_read_b128 v[136:139], v66 offset:10240\n"
    "ds_read_b64_tr_b16 v[184:185], v68 offset:20480\n"
    "ds_read_b64_tr_b16 v[186:187], v68 offset:20992\n"
    "s_waitcnt lgkmcnt(9)\n"
    "v_mfma_f32_32x32x16_bf16 v[160:175], v[188:191], v[140:143], v[160:175]\n"
    "ds_read_b128 v[140:143], v67 offset:11264\n"
    "ds_read_b64_tr_b16 v[188:189], v68 offset:22528\n"
    "ds_read_b64_tr_b16 v[190:191], v68 offset:23040\n"
    "s_waitcnt lgkmcnt(9)\n"
    "v_mfma_f32_32x32x16_bf16 v[160:175], v[176:179], v[128:131], v[160:175]\n"
    "ds_read_b128 v[128:131], v64 offset:12288\n"
    "ds_read_b64_tr_b16 v[176:177], v68 offset:24576\n"
    "ds_read_b64_tr_b16 v[178:179], v68 offset:25088\n"
    "s_waitcnt lgkmcnt(9)\n"
    "v_mfma_f32_32x32x16_bf16 v[160:175], v[180:183], v[132:135], v[160:175]\n"
    "ds_read_b128 v[132:135], v65 offset:13312\n"
    "ds_read_b64_tr_b16 v[180:181], v68 offset:26624\n"
    "ds_read_b64_tr_b16 v[182:183], v68 offset:27136\n"
    "s_waitcnt lgkmcnt(9)\n"
    "v_mfma_f32_32x32x16_bf16 v[160:175], v[184:187], v[136:139], v[160:175]\n"
    "ds_read_b128 v[136:139], v66 offset:14336\n"
    "ds_read_b64_tr_b16 v[184:185], v68 offset:28672\n"
    "ds_read_b64_tr_b16 v[186:187], v68 offset:29184\n"
    "s_waitcnt lgkmcnt(9)\n"
    "v_mfma_f32_32x32x16_bf16 v[160:175], v[188:191], v[140:143], v[160:175]\n"
    "ds_read_b128 v[140:143], v67 offset:15360\n"
    "ds_read_b64_tr_b16 v[188:189], v68 offset:30720\n"
    "ds_read_b64_tr_b16 v[190:191], v68 offset:31232\n"
    "s_waitcnt lgkmcnt(9)\n"
    "v_mfma_f32_32x32x16_bf16 v[160:175], v[176:179], v[128:131], v[160:175]\n"
    "ds_read_b128 v[128:131], v64 offset:16384\n"
    "ds_read_b64_tr_b16 v[176:177], v68 offset:32768\n"
    "ds_read_b64_tr_b16 v[178:179], v68 offset:33280\n"
    "s_waitcnt lgkmcnt(9)\n"
    "v_mfma_f32_32x32x16_bf16 v[160:175], v[180:183], v[132:135], v[160:175]\n"
    "ds_read_b128 v[132:135], v65 offset:17408\n"
    "ds_read_b64_tr_b16 v[180:181], v68 offset:34816\n"
    "ds_read_b64_tr_b16 v[182:183], v68 offset:35328\n"
    "s_waitcnt lgkmcnt(9)\n"
    "v_mfma_f32_32x32x16_bf16 v[160:175], v[184:187], v[136:139], v[160:175]\n"
    "ds_read_b128 v[136:139], v66 offset:18432\n"
    "ds_read_b64_tr_b16 v[184:185], v68 offset:36864\n"
    "ds_read_b64_tr_b16 v[186:187], v68 offset:37376\n"
    "s_waitcnt lgkmcnt(9)\n"
    "v_mfma_f32_32x32x16_bf16 v[160:175], v[188:191], v[140:143], v[160:175]\n"
    "ds_read_b128 v[140:143], v67 offset:19456\n"
    "ds_read_b64_tr_b16 v[188:189], v68 offset:38912\n"
    "ds_read_b64_tr_b16 v[190:191], v68 offset:39424\n"
    "s_waitcnt lgkmcnt(9)\n"
    "v_mfma_f32_32x32x16_bf16 v[160:175], v[176:179], v[128:131], v[160:175]\n"
    "ds_read_b128 v[128:131], v64 offset:20480\n"
    "ds_read_b64_tr_b16 v[176:177], v68 offset:40960\n"
    "ds_read_b64_tr_b16 v[178:179], v68 offset:41472\n"
    "s_waitcnt lgkmcnt(9)\n"
    "v_mfma_f32_32x32x16_bf16 v[160:175], v[180:183], v[132:135], v[160:175]\n"
    "ds_read_b128 v[132:135], v65 offset:21504\n"
    "ds_read_b64_tr_b16 v[180:181], v68 offset:43008\n"
    "ds_read_b64_tr_b16 v[182:183], v68 offset:43520\n"
    "s_waitcnt lgkmcnt(9)\n"
    "v_mfma_f32_32x32x16_bf16 v[160:175], v[184:187], v[136:139], v[160:175]\n"
    "ds_read_b128 v[136:139], v66 offset:22528\n"
    "ds_read_b64_tr_b16 v[184:185], v68 offset:45056\n"
    "ds_read_b64_tr_b16 v[186:187], v68 offset:45568\n"
    "s_waitcnt lgkmcnt(9)\n"
    "v_mfma_f32_32x32x16_bf16 v[160:175], v[188:191], v[140:143], v[160:175]\n"
    "ds_read_b128 v[140:143], v67 offset:23552\n"
    "ds_read_b64_tr_b16 v[188:189], v68 offset:47104\n"
    "ds_read_b64_tr_b16 v[190:191], v68 offset:47616\n"
    "s_waitcnt lgkmcnt(9)\n"
    "v_mfma_f32_32x32x16_bf16 v[160:175], v[176:179], v[128:131], v[160:175]\n"
    "ds_read_b128 v[128:131], v64 offset:24576\n"
    "ds_read_b64_tr_b16 v[176:177], v68 offset:49152\n"
    "ds_read_b64_tr_b16 v[178:179], v68 offset:49664\n"
    "s_waitcnt lgkmcnt(9)\n"
    "v_mfma_f32_32x32x16_bf16 v[160:175], v[180:183], v[132:135], v[160:175]\n"
    "ds_read_b128 v[132:135], v65 offset:25600\n"
    "ds_read_b64_tr_b16 v[180:181], v68 offset:51200\n"
    "ds_read_b64_tr_b16 v[182:183], v68 offset:51712\n"
    "s_waitcnt lgkmcnt(9)\n"
    "v_mfma_f32_32x32x16_bf16 v[160:175], v[184:187], v[136:139], v[160:175]\n"
    "ds_read_b128 v[136:139], v66 offset:26624\n"
    "ds_read_b64_tr_b16 v[184:185], v68 offset:53248\n"
    "ds_read_b64_tr_b16 v[186:187], v68 offset:53760\n"
    "s_waitcnt lgkmcnt(9)\n"
    "v_mfma_f32_32x32x16_bf16 v[160:175], v[188:191], v[140:143], v[160:175]\n"
    "ds_read_b128 v[140:143], v67 offset:27648\n"
    "ds_read_b64_tr_b16 v[188:189], v68 offset:55296\n"
    "ds_read_b64_tr_b16 v[190:191], v68 offset:55808\n"
    "s_waitcnt lgkmcnt(9)\n"
    "v_mfma_f32_32x32x16_bf16 v[160:175], v[176:179], v[128:131], v[160:175]\n"
    "ds_read_b128 v[128:131], v64 offset:28672\n"
    "ds_read_b64_tr_b16 v[176:177], v68 offset:57344\n"
    "ds_read_b64_tr_b16 v[178:179], v68 offset:57856\n"
    "s_waitcnt lgkmcnt(9)\n"
    "v_mfma_f32_32x32x16_bf16 v[160:175], v[180:183], v[132:135], v[160:175]\n"
    "ds_read_b128 v[132:135], v65 offset:29696\n"
    "ds_read_b64_tr_b16 v[180:181], v68 offset:59392\n"
    "ds_read_b64_tr_b16 v[182:183], v68 offset:59904\n"
    "s_waitcnt lgkmcnt(9)\n"
    "v_mfma_f32_32x32x16_bf16 v[160:175], v[184:187], v[136:139], v[160:175]\n"
    "ds_read_b128 v[136:139], v66 offset:30720\n"
    "ds_read_b64_tr_b16 v[184:185], v68 offset:61440\n"
    "ds_read_b64_tr_b16 v[186:187], v68 offset:61952\n"
    "s_waitcnt lgkmcnt(9)\n"
    "v_mfma_f32_32x32x16_bf16 v[160:175], v[188:191], v[140:143], v[160:175]\n"
    "ds_read_b128 v[140:143], v67 offset:31744\n"
    "ds_read_b64_tr_b16 v[188:189], v68 offset:63488\n"
    "ds_read_b64_tr_b16 v[190:191], v68 offset:64000\n"
    "s_waitcnt lgkmcnt(9)\n"
    "v_mfma_f32_32x32x16_bf16 v[160:175], v[176:179], v[128:131], v[160:175]\n"
    "s_waitcnt lgkmcnt(6)\n"
    "v_mfma_f32_32x32x16_bf16 v[160:175], v[180:183], v[132:135], v[160:175]\n"
    "s_waitcnt lgkmcnt(3)\n"
    "v_mfma_f32_32x32x16_bf16 v[160:175], v[184:187], v[136:139], v[160:175]\n"
    "s_waitcnt lgkmcnt(0)\n"
    "v_mfma_f32_32x32x16_bf16 v[160:175], v[188:191], v[140:143], v[160:175]\n"
    "s_setprio 1\n"
    "v_mul_f32 v246, 0x3eb8aa3b, v246\n"
    "s_nop 9\n"
    "v_pk_mul_f32 v[160:161], v[160:161], v[246:247] op_sel_hi:[1,0]\n"
    "v_pk_mul_f32 v[162:163], v[162:163], v[246:247] op_sel_hi:[1,0]\n"
    "v_pk_mul_f32 v[164:165], v[164:165], v[246:247] op_sel_hi:[1,0]\n"
    "v_pk_mul_f32 v[166:167], v[166:167], v[246:247] op_sel_hi:[1,0]\n"
    "v_pk_mul_f32 v[168:169], v[168:169], v[246:247] op_sel_hi:[1,0]\n"
    "v_pk_mul_f32 v[170:171], v[170:171], v[246:247] op_sel_hi:[1,0]\n"
    "v_pk_mul_f32 v[172:173], v[172:173], v[246:247] op_sel_hi:[1,0]\n"
    "v_pk_mul_f32 v[174:175], v[174:175], v[246:247] op_sel_hi:[1,0]\n"
    "v_cvt_pk_bf16_f32 v128, v160, v161\n"
    "v_cvt_pk_bf16_f32 v129, v162, v163\n"
    "v_cvt_pk_bf16_f32 v130, v164, v165\n"
    "v_cvt_pk_bf16_f32 v131, v166, v167\n"
    "ds_write_b128 v70, v[128:131]\n"
    "v_cvt_pk_bf16_f32 v132, v168, v169\n"
    "v_cvt_pk_bf16_f32 v133, v170, v171\n"
    "v_cvt_pk_bf16_f32 v134, v172, v173\n"
    "v_cvt_pk_bf16_f32 v135, v174, v175\n"
    "ds_write_b128 v70, v[132:135] offset:2048\n"
    "s_waitcnt lgkmcnt(0)\n"
    "s_mov_b64 exec, 1\n"
    "ds_add_u32 v240, v241 offset:0\n"
    "s_mov_b64 exec, -1\n"
    "s_mov_b32 s71, 8388608\n"
    ".Lkg4149_spin:\n"
    "ds_read_b32 v246, v240 offset:36\n"
    "s_waitcnt lgkmcnt(0)\n"
    "v_cmp_gt_u32 vcc, 4, v246\n"
    "s_cbranch_vccz .Lkg4149_go\n"
    "s_sleep 1\n"
    "s_sub_u32 s71, s71, 1\n"
    "s_cbranch_scc0 .Lkg4149_spin\n"
    ".Lkg4149_go:\n"
    "s_mov_b32 s71, 8388608\n"
    ".Lqg4159_spin:\n"
    "ds_read_b32 v246, v240\n"
    "s_waitcnt lgkmcnt(0)\n"
    "v_cmp_gt_u32 vcc, 4, v246\n"
    "s_cbranch_vccz .Lqg4159_go\n"
    "s_sub_u32 s71, s71, 1\n"
    "s_cbranch_scc0 .Lqg4159_spin\n"
    ".Lqg4159_go:\n"
    "s_add_u32 m0, s70, 0\n"
    "s_nop 0\n"
    "buffer_load_dwordx4 v238, s[32:35], s68 offen lds\n"
    "buffer_load_dwordx4 v239, s[32:35], s68 offen offset:1024 lds\n"
    "s_add_u32 m0, s70, 2048\n"
    "s_nop 0\n"
    "buffer_load_dwordx4 v238, s[36:39], s68 offen lds\n"
    "buffer_load_dwordx4 v239, s[36:39], s68 offen offset:1024 lds\n"
    "s_add_u32 s68, s68, s74\n"
    "s_add_u32 m0, s70, 4096\n"
    "s_waitcnt vmcnt(5)\n"
    "buffer_load_dwordx4 v238, s[32:35], s68 offen lds\n"
    "buffer_load_dwordx4 v239, s[32:35], s68 offen offset:1024 lds\n"
    "s_add_u32 m0, s70, 6144\n"
    "s_waitcnt vmcnt(5)\n"
    "buffer_load_dwordx4 v238, s[36:39], s68 offen lds\n"
    "buffer_load_dwordx4 v239, s[36:39], s68 offen offset:1024 lds\n"
    "s_add_u32 s68, s68, s74\n"
    "s_add_u32 m0, s70, 8192\n"
    "s_waitcnt vmcnt(5)\n"
    "buffer_load_dwordx4 v238, s[32:35], s68 offen lds\n"
    "buffer_load_dwordx4 v239, s[32:35], s68 offen offset:1024 lds\n"
    "s_add_u32 m0, s70, 10240\n"
    "s_waitcnt vmcnt(5)\n"
    "buffer_load_dwordx4 v238, s[36:39], s68 offen lds\n"
    "buffer_load_dwordx4 v239, s[36:39], s68 offen offset:1024 lds\n"
    "s_add_u32 s68, s68, s74\n"
    "s_add_u32 m0, s70, 12288\n"
    "s_waitcnt vmcnt(5)\n"
    "buffer_load_dwordx4 v238, s[32:35], s68 offen lds\n"
    "buffer_load_dwordx4 v239, s[32:35], s68 offen offset:1024 lds\n"
    "s_add_u32 m0, s70, 14336\n"
    "s_waitcnt vmcnt(5)\n"
    "buffer_load_dwordx4 v238, s[36:39], s68 offen lds\n"
    "buffer_load_dwordx4 v239, s[36:39], s68 offen offset:1024 lds\n"
    "s_add_u32 s68, s68, s74\n"
    "v_lshl_add_u32 v220, v237, 12, v234\n"
    "v_add_u32 v221, 15360, v220\n"
    "s_mov_b32 s40, s18\n"
    "s_and_b32 s41, s19, 0xffff\n"
    "s_mov_b32 s42, 1835008\n"
    "s_mov_b32 s43, 0x20000\n"
    "s_mov_b32 s44, s12\n"
    "s_and_b32 s45, s13, 0xffff\n"
    "s_mov_b32 s46, 524288\n"
    "s_mov_b32 s47, 0x20000\n"
    "s_mov_b32 s69, 1835008\n"
    "s_mul_i32 s64, s75, 3\n"
    "s_bitcmp1_b32 s75, 0\n"
    "s_cselect_b32 s62, 12288, 0\n"
    "s_and_b32 s67, s53, 3\n"
    "s_mul_i32 s67, s67, 2\n"
    "s_add_u32 s67, s67, s55\n"
    "s_sub_u32 s67, s67, s53\n"
    "s_add_u32 s67, s67, 7\n"
    "s_and_b32 s67, s67, 7\n"
    "s_cmp_eq_u32 s67, 7\n"
    "s_cselect_b32 s67, 0, s67\n"
    "s_add_u32 s65, s67, 1\n"
    "s_cmp_gt_u32 s65, 7\n"
    "s_addc_u32 s65, s65, s53\n"
    "s_and_b32 s65, s65, 7\n"
    "s_and_b32 s66, s65, 1\n"
    "s_mul_i32 s65, s65, 7\n"
    "s_add_u32 s65, s65, s66\n"
    "s_lshl_b32 s65, s65, 1\n"
    "s_add_u32 s65, s65, s64\n"
    "s_lshl_b32 s4, s65, 14\n"
    "s_sub_u32 s4, s4, s62\n"
    "s_lshl_b32 s65, s65, 11\n"
    "s_add_u32 s12, s65, s72\n"
    "s_add_u32 s65, s67, 2\n"
    "s_cmp_gt_u32 s65, 7\n"
    "s_addc_u32 s65, s65, s53\n"
    "s_and_b32 s65, s65, 7\n"
    "s_and_b32 s66, s65, 1\n"
    "s_mul_i32 s65, s65, 7\n"
    "s_add_u32 s65, s65, s66\n"
    "s_lshl_b32 s65, s65, 1\n"
    "s_add_u32 s65, s65, s64\n"
    "s_lshl_b32 s5, s65, 14\n"
    "s_sub_u32 s5, s5, s62\n"
    "s_lshl_b32 s65, s65, 11\n"
    "s_add_u32 s13, s65, s72\n"
    "s_add_u32 s65, s67, 3\n"
    "s_cmp_gt_u32 s65, 7\n"
    "s_addc_u32 s65, s65, s53\n"
    "s_and_b32 s65, s65, 7\n"
    "s_and_b32 s66, s65, 1\n"
    "s_mul_i32 s65, s65, 7\n"
    "s_add_u32 s65, s65, s66\n"
    "s_lshl_b32 s65, s65, 1\n"
    "s_add_u32 s65, s65, s64\n"
    "s_lshl_b32 s6, s65, 14\n"
    "s_sub_u32 s6, s6, s62\n"
    "s_lshl_b32 s65, s65, 11\n"
    "s_add_u32 s14, s65, s72\n"
    "s_add_u32 s65, s67, 4\n"
    "s_cmp_gt_u32 s65, 7\n"
    "s_addc_u32 s65, s65, s53\n"
    "s_and_b32 s65, s65, 7\n"
    "s_and_b32 s66, s65, 1\n"
    "s_mul_i32 s65, s65, 7\n"
    "s_add_u32 s65, s65, s66\n"
    "s_lshl_b32 s65, s65, 1\n"
    "s_add_u32 s65, s65, s64\n"
    "s_lshl_b32 s7, s65, 14\n"
    "s_sub_u32 s7, s7, s62\n"
    "s_lshl_b32 s65, s65, 11\n"
    "s_add_u32 s15, s65, s72\n"
    "s_add_u32 s65, s67, 5\n"
    "s_cmp_gt_u32 s65, 7\n"
    "s_addc_u32 s65, s65, s53\n"
    "s_and_b32 s65, s65, 7\n"
    "s_and_b32 s66, s65, 1\n"
    "s_mul_i32 s65, s65, 7\n"
    "s_add_u32 s65, s65, s66\n"
    "s_lshl_b32 s65, s65, 1\n"
    "s_add_u32 s65, s65, s64\n"
    "s_lshl_b32 s8, s65, 14\n"
    "s_sub_u32 s8, s8, s62\n"
    "s_lshl_b32 s65, s65, 11\n"
    "s_add_u32 s16, s65, s72\n"
    "s_add_u32 s65, s67, 6\n"
    "s_cmp_gt_u32 s65, 7\n"
    "s_addc_u32 s65, s65, s53\n"
    "s_and_b32 s65, s65, 7\n"
    "s_and_b32 s66, s65, 1\n"
    "s_mul_i32 s65, s65, 7\n"
    "s_add_u32 s65, s65, s66\n"
    "s_lshl_b32 s65, s65, 1\n"
    "s_add_u32 s65, s65, s64\n"
    "s_lshl_b32 s9, s65, 14\n"
    "s_sub_u32 s9, s9, s62\n"
    "s_lshl_b32 s65, s65, 11\n"
    "s_add_u32 s17, s65, s72\n"
    "s_add_u32 s65, s67, 7\n"
    "s_cmp_gt_u32 s65, 7\n"
    "s_addc_u32 s65, s65, s53\n"
    "s_and_b32 s65, s65, 7\n"
    "s_and_b32 s66, s65, 1\n"
    "s_mul_i32 s65, s65, 7\n"
    "s_add_u32 s65, s65, s66\n"
    "s_lshl_b32 s65, s65, 1\n"
    "s_add_u32 s65, s65, s64\n"
    "s_lshl_b32 s10, s65, 14\n"
    "s_sub_u32 s10, s10, s62\n"
    "s_lshl_b32 s65, s65, 11\n"
    "s_add_u32 s18, s65, s72\n"
    "s_lshl_b32 s62, s62, 1\n"
    "s_add_u32 s62, s62, 4096\n"
    "s_lshl_b32 s23, s52, 2\n"
    "s_add_u32 s23, s23, s75\n"
    "s_lshl_b32 s23, s23, 14\n"
    "s_add_u32 s23, s23, 1048576\n"
    "s_lshl_b32 s78, s52, 1\n"
    "s_and_b32 s64, s79, 1\n"
    "s_add_u32 s78, s78, s64\n"
    "s_lshl_b32 s78, s78, 7\n"
    "s_lshl_b32 s11, s52, 3\n"
    "s_add_u32 s11, s11, s53\n"
    "s_lshl_b32 s11, s11, 2\n"
    "s_add_u32 s11, s11, s75\n"
    "s_lshl_b32 s11, s11, 11\n"
    "s_add_u32 s11, s11, 1048576\n"
    "s_lshl_b32 s76, s75, 13\n"
    "s_add_u32 s76, s76, 131072\n"
    "s_add_u32 s67, s55, s53\n"
    "s_add_u32 s66, s67, 0\n"
    "s_and_b32 s66, s66, 3\n"
    "s_mul_i32 s66, s66, 28\n"
    "s_add_u32 s66, s66, 12\n"
    "s_lshl_b32 s96, s66, 14\n"
    "s_lshl_b32 s66, s66, 11\n"
    "s_add_u32 s19, s66, s72\n"
    "s_add_u32 s66, s67, 1\n"
    "s_and_b32 s66, s66, 3\n"
    "s_mul_i32 s66, s66, 28\n"
    "s_add_u32 s66, s66, 12\n"
    "s_lshl_b32 s97, s66, 14\n"
    "s_lshl_b32 s66, s66, 11\n"
    "s_add_u32 s20, s66, s72\n"
    "s_add_u32 s66, s67, 2\n"
    "s_and_b32 s66, s66, 3\n"
    "s_mul_i32 s66, s66, 28\n"
    "s_add_u32 s66, s66, 12\n"
    "s_lshl_b32 s98, s66, 14\n"
    "s_lshl_b32 s66, s66, 11\n"
    "s_add_u32 s21, s66, s72\n"
    "s_add_u32 s66, s67, 3\n"
    "s_and_b32 s66, s66, 3\n"
    "s_mul_i32 s66, s66, 28\n"
    "s_add_u32 s66, s66, 12\n"
    "s_lshl_b32 s99, s66, 14\n"
    "s_lshl_b32 s66, s66, 11\n"
    "s_add_u32 s22, s66, s72\n"
    "s_mov_b32 s42, 0\n"
    "s_and_b32 s59, s53, 1\n"
    "s_mul_i32 s58, s53, 7\n"
    "s_add_u32 s58, s58, s59\n"
    "s_lshl_b32 s60, s58, 15\n"
    "s_mov_b32 s58, s60\n"
    "s_mov_b32 s59, 131072\n"
    "s_mov_b32 s71, 8388608\n"
    ".Lqr4371_spin:\n"
    "ds_read_b32 v246, v240 offset:20\n"
    "s_waitcnt lgkmcnt(0)\n"
    "v_cmp_gt_u32 vcc, 4, v246\n"
    "s_cbranch_vccz .Lqr4371_go\n"
    "s_sleep 1\n"
    "s_sub_u32 s71, s71, 1\n"
    "s_cbranch_scc0 .Lqr4371_spin\n"
    ".Lqr4371_go:\n"
    "s_setprio 0\n"
    "s_waitcnt vmcnt(14)\n"
    "ds_read_b128 v[160:163], v228\n"
    "ds_read_b128 v[164:167], v228 offset:256\n"
    "ds_read_b128 v[168:171], v228 offset:32768\n"
    "ds_read_b128 v[172:175], v228 offset:33024\n"
    "ds_read_b64_tr_b16 v[128:129], v230\n"
    "ds_read_b64_tr_b16 v[130:131], v230 offset:256\n"
    "ds_read_b64_tr_b16 v[132:133], v231\n"
    "ds_read_b64_tr_b16 v[134:135], v231 offset:256\n"
    "s_mov_b32 s71, 3\n"
    ".p2align 6\n"
    ".Lg3_loop:\n"
    "s_add_u32 m0, s70, 0\n"
    "s_waitcnt vmcnt(12) lgkmcnt(0)\n"
    "v_mfma_f32_16x16x32_bf16 v[0:3], v[128:131], v[160:163], v[0:3]\n"
    "ds_read_b64_tr_b16 v[144:145], v230 offset:2048\n"
    "ds_read_b64_tr_b16 v[146:147], v230 offset:2304\n"
    "v_mfma_f32_16x16x32_bf16 v[4:7], v[128:131], v[164:167], v[4:7]\n"
    "ds_read_b64_tr_b16 v[148:149], v231 offset:2048\n"
    "v_mfma_f32_16x16x32_bf16 v[8:11], v[128:131], v[168:171], v[8:11]\n"
    "ds_read_b64_tr_b16 v[150:151], v231 offset:2304\n"
    "ds_read_b128 v[176:179], v229 offset:2048\n"
    "v_mfma_f32_16x16x32_bf16 v[12:15], v[128:131], v[172:175], v[12:15]\n"
    "ds_read_b128 v[180:183], v229 offset:2304\n"
    "v_mfma_f32_16x16x32_bf16 v[16:19], v[132:135], v[160:163], v[16:19]\n"
    "s_waitcnt vmcnt(5)\n"
    "buffer_load_dwordx4 v238, s[32:35], s68 offen lds\n"
    "v_mfma_f32_16x16x32_bf16 v[20:23], v[132:135], v[164:167], v[20:23]\n"
    "buffer_load_dwordx4 v239, s[32:35], s68 offen offset:1024 lds\n"
    "s_mov_b32 m0, s59\n"
    "v_mfma_f32_16x16x32_bf16 v[24:27], v[132:135], v[168:171], v[24:27]\n"
    "buffer_load_dwordx4 v220, s[40:43], s58 offen lds\n"
    "v_mfma_f32_16x16x32_bf16 v[28:31], v[132:135], v[172:175], v[28:31]\n"
    "s_add_u32 m0, s70, 2048\n"
    "s_waitcnt vmcnt(12) lgkmcnt(0)\n"
    "v_mfma_f32_16x16x32_bf16 v[32:35], v[144:147], v[160:163], v[32:35]\n"
    "ds_read_b64_tr_b16 v[128:129], v230 offset:4096\n"
    "ds_read_b64_tr_b16 v[130:131], v230 offset:4352\n"
    "v_mfma_f32_16x16x32_bf16 v[36:39], v[144:147], v[164:167], v[36:39]\n"
    "ds_read_b64_tr_b16 v[132:133], v231 offset:4096\n"
    "v_mfma_f32_16x16x32_bf16 v[40:43], v[144:147], v[168:171], v[40:43]\n"
    "ds_read_b64_tr_b16 v[134:135], v231 offset:4352\n"
    "ds_read_b128 v[184:187], v229 offset:34816\n"
    "v_mfma_f32_16x16x32_bf16 v[44:47], v[144:147], v[172:175], v[44:47]\n"
    "ds_read_b128 v[188:191], v229 offset:35072\n"
    "v_mfma_f32_16x16x32_bf16 v[48:51], v[148:151], v[160:163], v[48:51]\n"
    "s_waitcnt vmcnt(5)\n"
    "buffer_load_dwordx4 v238, s[36:39], s68 offen lds\n"
    "v_mfma_f32_16x16x32_bf16 v[52:55], v[148:151], v[164:167], v[52:55]\n"
    "buffer_load_dwordx4 v239, s[36:39], s68 offen offset:1024 lds\n"
    "s_mov_b32 m0, s59\n"
    "v_mfma_f32_16x16x32_bf16 v[56:59], v[148:151], v[168:171], v[56:59]\n"
    "buffer_load_dwordx4 v221, s[40:43], s58 offen offset:1024 lds\n"
    "s_add_u32 s58, s58, 4096\n"
    "s_add_u32 s59, s59, 2048\n"
    "v_mfma_f32_16x16x32_bf16 v[60:63], v[148:151], v[172:175], v[60:63]\n"
    "s_add_u32 s68, s68, s74\n"
    "s_add_u32 m0, s70, 4096\n"
    "s_waitcnt vmcnt(12) lgkmcnt(0)\n"
    "v_mfma_f32_16x16x32_bf16 v[0:3], v[128:131], v[176:179], v[0:3]\n"
    "ds_read_b64_tr_b16 v[144:145], v230 offset:6144\n"
    "ds_read_b64_tr_b16 v[146:147], v230 offset:6400\n"
    "v_mfma_f32_16x16x32_bf16 v[4:7], v[128:131], v[180:183], v[4:7]\n"
    "ds_read_b64_tr_b16 v[148:149], v231 offset:6144\n"
    "v_mfma_f32_16x16x32_bf16 v[8:11], v[128:131], v[184:187], v[8:11]\n"
    "ds_read_b64_tr_b16 v[150:151], v231 offset:6400\n"
    "ds_read_b128 v[160:163], v228 offset:4096\n"
    "v_mfma_f32_16x16x32_bf16 v[12:15], v[128:131], v[188:191], v[12:15]\n"
    "ds_read_b128 v[164:167], v228 offset:4352\n"
    "v_mfma_f32_16x16x32_bf16 v[16:19], v[132:135], v[176:179], v[16:19]\n"
    "s_waitcnt vmcnt(5)\n"
    "buffer_load_dwordx4 v238, s[32:35], s68 offen lds\n"
    "v_mfma_f32_16x16x32_bf16 v[20:23], v[132:135], v[180:183], v[20:23]\n"
    "buffer_load_dwordx4 v239, s[32:35], s68 offen offset:1024 lds\n"
    "s_mov_b32 m0, s59\n"
    "v_mfma_f32_16x16x32_bf16 v[24:27], v[132:135], v[184:187], v[24:27]\n"
    "buffer_load_dwordx4 v220, s[40:43], s58 offen lds\n"
    "v_mfma_f32_16x16x32_bf16 v[28:31], v[132:135], v[188:191], v[28:31]\n"
    "s_add_u32 m0, s70, 6144\n"
    "s_waitcnt vmcnt(12) lgkmcnt(0)\n"
    "v_mfma_f32_16x16x32_bf16 v[32:35], v[144:147], v[176:179], v[32:35]\n"
    "ds_read_b64_tr_b16 v[128:129], v230 offset:8192\n"
    "ds_read_b64_tr_b16 v[130:131], v230 offset:8448\n"
    "v_mfma_f32_16x16x32_bf16 v[36:39], v[144:147], v[180:183], v[36:39]\n"
    "ds_read_b64_tr_b16 v[132:133], v231 offset:8192\n"
    "v_mfma_f32_16x16x32_bf16 v[40:43], v[144:147], v[184:187], v[40:43]\n"
    "ds_read_b64_tr_b16 v[134:135], v231 offset:8448\n"
    "ds_read_b128 v[168:171], v228 offset:36864\n"
    "v_mfma_f32_16x16x32_bf16 v[44:47], v[144:147], v[188:191], v[44:47]\n"
    "ds_read_b128 v[172:175], v228 offset:37120\n"
    "v_mfma_f32_16x16x32_bf16 v[48:51], v[148:151], v[176:179], v[48:51]\n"
    "s_waitcnt vmcnt(5)\n"
    "buffer_load_dwordx4 v238, s[36:39], s68 offen lds\n"
    "v_mfma_f32_16x16x32_bf16 v[52:55], v[148:151], v[180:183], v[52:55]\n"
    "buffer_load_dwordx4 v239, s[36:39], s68 offen offset:1024 lds\n"
    "s_mov_b32 m0, s59\n"
    "v_mfma_f32_16x16x32_bf16 v[56:59], v[148:151], v[184:187], v[56:59]\n"
    "buffer_load_dwordx4 v221, s[40:43], s58 offen offset:1024 lds\n"
    "s_add_u32 s58, s58, 28672\n"
    "s_add_u32 s59, s59, 2048\n"
    "v_mfma_f32_16x16x32_bf16 v[60:63], v[148:151], v[188:191], v[60:63]\n"
    "s_add_u32 s68, s68, s74\n"
    "s_add_u32 m0, s70, 8192\n"
    "s_waitcnt vmcnt(12) lgkmcnt(0)\n"
    "v_mfma_f32_16x16x32_bf16 v[0:3], v[128:131], v[160:163], v[0:3]\n"
    "ds_read_b64_tr_b16 v[144:145], v230 offset:10240\n"
    "ds_read_b64_tr_b16 v[146:147], v230 offset:10496\n"
    "v_mfma_f32_16x16x32_bf16 v[4:7], v[128:131], v[164:167], v[4:7]\n"
    "ds_read_b64_tr_b16 v[148:149], v231 offset:10240\n"
    "v_mfma_f32_16x16x32_bf16 v[8:11], v[128:131], v[168:171], v[8:11]\n"
    "ds_read_b64_tr_b16 v[150:151], v231 offset:10496\n"
    "ds_read_b128 v[176:179], v229 offset:6144\n"
    "v_mfma_f32_16x16x32_bf16 v[12:15], v[128:131], v[172:175], v[12:15]\n"
    "ds_read_b128 v[180:183], v229 offset:6400\n"
    "v_mfma_f32_16x16x32_bf16 v[16:19], v[132:135], v[160:163], v[16:19]\n"
    "s_waitcnt vmcnt(5)\n"
    "buffer_load_dwordx4 v238, s[32:35], s68 offen lds\n"
    "v_mfma_f32_16x16x32_bf16 v[20:23], v[132:135], v[164:167], v[20:23]\n"
    "buffer_load_dwordx4 v239, s[32:35], s68 offen offset:1024 lds\n"
    "s_mov_b32 m0, s59\n"
    "v_mfma_f32_16x16x32_bf16 v[24:27], v[132:135], v[168:171], v[24:27]\n"
    "buffer_load_dwordx4 v220, s[40:43], s58 offen lds\n"
    "v_mfma_f32_16x16x32_bf16 v[28:31], v[132:135], v[172:175], v[28:31]\n"
    "s_add_u32 m0, s70, 10240\n"
    "s_waitcnt vmcnt(12) lgkmcnt(0)\n"
    "v_mfma_f32_16x16x32_bf16 v[32:35], v[144:147], v[160:163], v[32:35]\n"
    "ds_read_b64_tr_b16 v[128:129], v230 offset:12288\n"
    "ds_read_b64_tr_b16 v[130:131], v230 offset:12544\n"
    "v_mfma_f32_16x16x32_bf16 v[36:39], v[144:147], v[164:167], v[36:39]\n"
    "ds_read_b64_tr_b16 v[132:133], v231 offset:12288\n"
    "v_mfma_f32_16x16x32_bf16 v[40:43], v[144:147], v[168:171], v[40:43]\n"
    "ds_read_b64_tr_b16 v[134:135], v231 offset:12544\n"
    "ds_read_b128 v[184:187], v229 offset:38912\n"
    "v_mfma_f32_16x16x32_bf16 v[44:47], v[144:147], v[172:175], v[44:47]\n"
    "ds_read_b128 v[188:191], v229 offset:39168\n"
    "v_mfma_f32_16x16x32_bf16 v[48:51], v[148:151], v[160:163], v[48:51]\n"
    "s_waitcnt vmcnt(5)\n"
    "buffer_load_dwordx4 v238, s[36:39], s68 offen lds\n"
    "v_mfma_f32_16x16x32_bf16 v[52:55], v[148:151], v[164:167], v[52:55]\n"
    "buffer_load_dwordx4 v239, s[36:39], s68 offen offset:1024 lds\n"
    "s_mov_b32 m0, s59\n"
    "v_mfma_f32_16x16x32_bf16 v[56:59], v[148:151], v[168:171], v[56:59]\n"
    "buffer_load_dwordx4 v221, s[40:43], s58 offen offset:1024 lds\n"
    "s_add_u32 s58, s58, 4096\n"
    "s_add_u32 s59, s59, 2048\n"
    "v_mfma_f32_16x16x32_bf16 v[60:63], v[148:151], v[172:175], v[60:63]\n"
    "s_add_u32 s68, s68, s74\n"
    "s_add_u32 m0, s70, 12288\n"
    "s_waitcnt vmcnt(12) lgkmcnt(0)\n"
    "v_mfma_f32_16x16x32_bf16 v[0:3], v[128:131], v[176:179], v[0:3]\n"
    "ds_read_b64_tr_b16 v[144:145], v230 offset:14336\n"
    "ds_read_b64_tr_b16 v[146:147], v230 offset:14592\n"
    "v_mfma_f32_16x16x32_bf16 v[4:7], v[128:131], v[180:183], v[4:7]\n"
    "ds_read_b64_tr_b16 v[148:149], v231 offset:14336\n"
    "v_mfma_f32_16x16x32_bf16 v[8:11], v[128:131], v[184:187], v[8:11]\n"
    "ds_read_b64_tr_b16 v[150:151], v231 offset:14592\n"
    "ds_read_b128 v[160:163], v228 offset:8192\n"
    "v_mfma_f32_16x16x32_bf16 v[12:15], v[128:131], v[188:191], v[12:15]\n"
    "ds_read_b128 v[164:167], v228 offset:8448\n"
    "v_mfma_f32_16x16x32_bf16 v[16:19], v[132:135], v[176:179], v[16:19]\n"
    "s_waitcnt vmcnt(5)\n"
    "buffer_load_dwordx4 v238, s[32:35], s68 offen lds\n"
    "v_mfma_f32_16x16x32_bf16 v[20:23], v[132:135], v[180:183], v[20:23]\n"
    "buffer_load_dwordx4 v239, s[32:35], s68 offen offset:1024 lds\n"
    "s_mov_b32 m0, s59\n"
    "v_mfma_f32_16x16x32_bf16 v[24:27], v[132:135], v[184:187], v[24:27]\n"
    "buffer_load_dwordx4 v220, s[40:43], s58 offen lds\n"
    "v_mfma_f32_16x16x32_bf16 v[28:31], v[132:135], v[188:191], v[28:31]\n"
    "s_add_u32 m0, s70, 14336\n"
    "s_waitcnt vmcnt(12) lgkmcnt(0)\n"
    "v_mfma_f32_16x16x32_bf16 v[32:35], v[144:147], v[176:179], v[32:35]\n"
    "ds_read_b64_tr_b16 v[128:129], v230\n"
    "ds_read_b64_tr_b16 v[130:131], v230 offset:256\n"
    "v_mfma_f32_16x16x32_bf16 v[36:39], v[144:147], v[180:183], v[36:39]\n"
    "ds_read_b64_tr_b16 v[132:133], v231\n"
    "v_mfma_f32_16x16x32_bf16 v[40:43], v[144:147], v[184:187], v[40:43]\n"
    "ds_read_b64_tr_b16 v[134:135], v231 offset:256\n"
    "ds_read_b128 v[168:171], v228 offset:40960\n"
    "v_mfma_f32_16x16x32_bf16 v[44:47], v[144:147], v[188:191], v[44:47]\n"
    "ds_read_b128 v[172:175], v228 offset:41216\n"
    "v_mfma_f32_16x16x32_bf16 v[48:51], v[148:151], v[176:179], v[48:51]\n"
    "s_waitcnt vmcnt(5)\n"
    "buffer_load_dwordx4 v238, s[36:39], s68 offen lds\n"
    "v_mfma_f32_16x16x32_bf16 v[52:55], v[148:151], v[180:183], v[52:55]\n"
    "buffer_load_dwordx4 v239, s[36:39], s68 offen offset:1024 lds\n"
    "s_mov_b32 m0, s59\n"
    "v_mfma_f32_16x16x32_bf16 v[56:59], v[148:151], v[184:187], v[56:59]\n"
    "buffer_load_dwordx4 v221, s[40:43], s58 offen offset:1024 lds\n"
    "s_add_u32 s58, s58, 28672\n"
    "s_add_u32 s59, s59, 2048\n"
    "v_mfma_f32_16x16x32_bf16 v[60:63], v[148:151], v[188:191], v[60:63]\n"
    "s_setprio 1\n"
    "v_add_u32 v228, 8192, v228\n"
    "v_add_u32 v229, 8192, v229\n"
    "s_setprio 0\n"
    "s_add_u32 s68, s68, s74\n"
    "s_cmp_lg_u32 s68, s69\n"
    "s_cbranch_scc1 .Lg3_nosw\n"
    "s_mov_b64 s[32:33], s[44:45]\n"
    "s_mov_b32 s34, s46\n"
    "s_add_u32 s36, s44, 16384\n"
    "s_addc_u32 s37, s45, 0\n"
    "s_sub_u32 s38, s46, 16384\n"
    "s_lshl_b32 s68, s75, 17\n"
    "s_add_u32 s69, s68, 0x20000\n"
    "s_mov_b32 s74, 32768\n"
    "s_setprio 1\n"
    "v_add_u32 v238, 0, v234\n"
    "v_add_u32 v239, 7168, v234\n"
    "s_setprio 0\n"
    ".Lg3_nosw:\n"
    "s_cmp_eq_u32 s71, 3\n"
    "s_cselect_b32 s58, s60, s58\n"
    "s_cselect_b32 s59, 131072, s59\n"
    "s_mov_b32 s42, 1835008\n"
    "s_cmp_lg_u32 s71, 1\n"
    "s_cbranch_scc1 .Lg3_nomark\n"
    "buffer_store_dwordx4 v[248:251], v242, s[28:31], s11 offen sc1\n"
    "buffer_store_dwordx4 v[248:251], v242, s[28:31], s11 offen offset:1024 sc1\n"
    ".Lg3_nomark:\n"
    "s_sub_u32 s71, s71, 1\n"
    "s_cbranch_scc0 .Lg3_loop\n"
    "s_setprio 1\n"
    "s_mul_i32 s58, s53, 14\n"
    "s_bitcmp1_b32 s53, 0\n"
    "s_cselect_b32 s59, 0, 12\n"
    "s_add_u32 s58, s58, s59\n"
    "s_lshl_b32 s58, s58, 11\n"
    "s_add_u32 s58, s58, s72\n"
    "s_waitcnt lgkmcnt(0)\n"
    "v_add_u32 v232, s70, v245\n"
    "v_xor_b32 v233, 64, v232\n"
    "v_lshl_add_u32 v234, v237, 12, v234\n"
    "v_add_u32 v235, 15360, v234\n"
    "v_add_u32 v238, s76, v245\n"
    "v_xor_b32 v239, 64, v238\n"
    "v_max_f32 v0, 0, v0\n"
    "v_max_f32 v1, 0, v1\n"
    "v_max_f32 v2, 0, v2\n"
    "v_max_f32 v3, 0, v3\n"
    "v_max_f32 v4, 0, v4\n"
    "v_max_f32 v5, 0, v5\n"
    "v_max_f32 v6, 0, v6\n"
    "v_max_f32 v7, 0, v7\n"
    "v_max_f32 v16, 0, v16\n"
    "v_max_f32 v17, 0, v17\n"
    "v_max_f32 v18, 0, v18\n"
    "v_max_f32 v19, 0, v19\n"
    "v_max_f32 v20, 0, v20\n"
    "v_max_f32 v21, 0, v21\n"
    "v_max_f32 v22, 0, v22\n"
    "v_max_f32 v23, 0, v23\n"
    "v_pk_mul_f32 v[0:1], v[0:1], v[32:33]\n"
    "v_pk_mul_f32 v[2:3], v[2:3], v[34:35]\n"
    "v_pk_mul_f32 v[4:5], v[4:5], v[36:37]\n"
    "v_pk_mul_f32 v[6:7], v[6:7], v[38:39]\n"
    "v_pk_mul_f32 v[16:17], v[16:17], v[48:49]\n"
    "v_pk_mul_f32 v[18:19], v[18:19], v[50:51]\n"
    "v_pk_mul_f32 v[20:21], v[20:21], v[52:53]\n"
    "v_pk_mul_f32 v[22:23], v[22:23], v[54:55]\n"
    "v_cvt_pk_bf16_f32 v128, v0, v1\n"
    "v_cvt_pk_bf16_f32 v129, v2, v3\n"
    "v_cvt_pk_bf16_f32 v132, v4, v5\n"
    "v_cvt_pk_bf16_f32 v133, v6, v7\n"
    "v_cvt_pk_bf16_f32 v130, v16, v17\n"
    "v_cvt_pk_bf16_f32 v131, v18, v19\n"
    "v_cvt_pk_bf16_f32 v134, v20, v21\n"
    "v_cvt_pk_bf16_f32 v135, v22, v23\n"
    "v_permlane16_swap_b32 v128, v132\n"
    "v_permlane16_swap_b32 v129, v133\n"
    "v_permlane16_swap_b32 v130, v134\n"
    "v_permlane16_swap_b32 v131, v135\n"
    "buffer_store_dwordx4 v[128:131], v242, s[28:31], s58 offen sc1\n"
    "buffer_store_dwordx4 v[132:135], v242, s[28:31], s58 offen offset:2048 sc1\n"
    "v_max_f32 v8, 0, v8\n"
    "v_max_f32 v9, 0, v9\n"
    "v_max_f32 v10, 0, v10\n"
    "v_max_f32 v11, 0, v11\n"
    "v_max_f32 v12, 0, v12\n"
    "v_max_f32 v13, 0, v13\n"
    "v_max_f32 v14, 0, v14\n"
    "v_max_f32 v15, 0, v15\n"
    "v_max_f32 v24, 0, v24\n"
    "v_max_f32 v25, 0, v25\n"
    "v_max_f32 v26, 0, v26\n"
    "v_max_f32 v27, 0, v27\n"
    "v_max_f32 v28, 0, v28\n"
    "v_max_f32 v29, 0, v29\n"
    "v_max_f32 v30, 0, v30\n"
    "v_max_f32 v31, 0, v31\n"
    "v_pk_mul_f32 v[8:9], v[8:9], v[40:41]\n"
    "v_pk_mul_f32 v[10:11], v[10:11], v[42:43]\n"
    "v_pk_mul_f32 v[12:13], v[12:13], v[44:45]\n"
    "v_pk_mul_f32 v[14:15], v[14:15], v[46:47]\n"
    "v_pk_mul_f32 v[24:25], v[24:25], v[56:57]\n"
    "v_pk_mul_f32 v[26:27], v[26:27], v[58:59]\n"
    "v_pk_mul_f32 v[28:29], v[28:29], v[60:61]\n"
    "v_pk_mul_f32 v[30:31], v[30:31], v[62:63]\n"
    "v_cvt_pk_bf16_f32 v136, v8, v9\n"
    "v_cvt_pk_bf16_f32 v137, v10, v11\n"
    "v_cvt_pk_bf16_f32 v140, v12, v13\n"
    "v_cvt_pk_bf16_f32 v141, v14, v15\n"
    "v_cvt_pk_bf16_f32 v138, v24, v25\n"
    "v_cvt_pk_bf16_f32 v139, v26, v27\n"
    "v_cvt_pk_bf16_f32 v142, v28, v29\n"
    "v_cvt_pk_bf16_f32 v143, v30, v31\n"
    "v_permlane16_swap_b32 v136, v140\n"
    "v_permlane16_swap_b32 v137, v141\n"
    "v_permlane16_swap_b32 v138, v142\n"
    "v_permlane16_swap_b32 v139, v143\n"
    "buffer_store_dwordx4 v[136:139], v242, s[28:31], s58 offen offset:1024 sc1\n"
    "buffer_store_dwordx4 v[140:143], v242, s[28:31], s58 offen offset:3072 sc1\n"
    "s_waitcnt vmcnt(4)\n"
    "ds_read_b64_tr_b16 v[160:161], v232\n"
    "ds_read_b64_tr_b16 v[162:163], v232 offset:1024\n"
    "ds_read_b64_tr_b16 v[164:165], v233\n"
    "ds_read_b64_tr_b16 v[166:167], v233 offset:1024\n"
    "ds_read_b64_tr_b16 v[168:169], v232 offset:2048\n"
    "ds_read_b64_tr_b16 v[170:171], v232 offset:3072\n"
    "ds_read_b64_tr_b16 v[172:173], v233 offset:2048\n"
    "ds_read_b64_tr_b16 v[174:175], v233 offset:3072\n"
    "s_add_u32 m0, s70, 0\n"
    "s_add_u32 s68, s96, 0\n"
    "s_waitcnt vmcnt(63) lgkmcnt(4)\n"
    "buffer_load_dwordx4 v234, s[40:43], s68 offen lds\n"
    "buffer_load_dwordx4 v235, s[40:43], s68 offen offset:1024 lds\n"
    "ds_read_b64_tr_b16 v[176:177], v232 offset:4096\n"
    "ds_read_b64_tr_b16 v[178:179], v232 offset:5120\n"
    "ds_read_b64_tr_b16 v[180:181], v233 offset:4096\n"
    "ds_read_b64_tr_b16 v[182:183], v233 offset:5120\n"
    "s_add_u32 m0, s70, 2048\n"
    "s_add_u32 s68, s96, 4096\n"
    "s_waitcnt vmcnt(63) lgkmcnt(4)\n"
    "buffer_load_dwordx4 v234, s[40:43], s68 offen lds\n"
    "buffer_load_dwordx4 v235, s[40:43], s68 offen offset:1024 lds\n"
    "ds_read_b64_tr_b16 v[184:185], v232 offset:6144\n"
    "ds_read_b64_tr_b16 v[186:187], v232 offset:7168\n"
    "ds_read_b64_tr_b16 v[188:189], v233 offset:6144\n"
    "ds_read_b64_tr_b16 v[190:191], v233 offset:7168\n"
    "s_add_u32 m0, s70, 4096\n"
    "s_add_u32 s68, s96, 32768\n"
    "s_waitcnt vmcnt(63) lgkmcnt(4)\n"
    "buffer_load_dwordx4 v234, s[40:43], s68 offen lds\n"
    "buffer_load_dwordx4 v235, s[40:43], s68 offen offset:1024 lds\n"
    "s_waitcnt vmcnt(6)\n"
    "s_add_u32 s64, s73, 24576\n"
    "s_mov_b64 exec, 3\n"
    "buffer_store_dwordx2 v[224:225], v227, s[28:31], s64 offen sc1\n"
    "s_mov_b64 exec, -1\n"
    "s_mov_b64 exec, 1\n"
    "ds_add_u32 v240, v241 offset:8\n"
    "s_mov_b64 exec, -1\n"
    "s_waitcnt lgkmcnt(0)\n"
    "ds_read_b64_tr_b16 v[192:193], v232 offset:8192\n"
    "ds_read_b64_tr_b16 v[194:195], v232 offset:9216\n"
    "ds_read_b64_tr_b16 v[196:197], v233 offset:8192\n"
    "ds_read_b64_tr_b16 v[198:199], v233 offset:9216\n"
    "s_add_u32 m0, s70, 6144\n"
    "s_add_u32 s68, s96, 36864\n"
    "s_waitcnt vmcnt(63) lgkmcnt(4)\n"
    "buffer_load_dwordx4 v234, s[40:43], s68 offen lds\n"
    "buffer_load_dwordx4 v235, s[40:43], s68 offen offset:1024 lds\n"
    "ds_read_b64_tr_b16 v[200:201], v232 offset:10240\n"
    "ds_read_b64_tr_b16 v[202:203], v232 offset:11264\n"
    "ds_read_b64_tr_b16 v[204:205], v233 offset:10240\n"
    "ds_read_b64_tr_b16 v[206:207], v233 offset:11264\n"
    "s_add_u32 m0, s70, 8192\n"
    "s_add_u32 s68, s97, 0\n"
    "s_waitcnt vmcnt(8) lgkmcnt(4)\n"
    "buffer_load_dwordx4 v234, s[40:43], s68 offen lds\n"
    "buffer_load_dwordx4 v235, s[40:43], s68 offen offset:1024 lds\n"
    "ds_read_b64_tr_b16 v[208:209], v232 offset:12288\n"
    "ds_read_b64_tr_b16 v[210:211], v232 offset:13312\n"
    "ds_read_b64_tr_b16 v[212:213], v233 offset:12288\n"
    "ds_read_b64_tr_b16 v[214:215], v233 offset:13312\n"
    "s_add_u32 m0, s70, 10240\n"
    "s_add_u32 s68, s97, 4096\n"
    "s_waitcnt vmcnt(8) lgkmcnt(4)\n"
    "buffer_load_dwordx4 v234, s[40:43], s68 offen lds\n"
    "buffer_load_dwordx4 v235, s[40:43], s68 offen offset:1024 lds\n"
    "ds_read_b64_tr_b16 v[216:217], v232 offset:14336\n"
    "ds_read_b64_tr_b16 v[218:219], v232 offset:15360\n"
    "ds_read_b64_tr_b16 v[220:221], v233 offset:14336\n"
    "ds_read_b64_tr_b16 v[222:223], v233 offset:15360\n"
    "s_add_u32 m0, s70, 12288\n"
    "s_add_u32 s68, s97, 32768\n"
    "s_waitcnt vmcnt(8) lgkmcnt(4)\n"
    "buffer_load_dwordx4 v234, s[40:43], s68 offen lds\n"
    "buffer_load_dwordx4 v235, s[40:43], s68 offen offset:1024 lds\n"
    "s_add_u32 m0, s70, 14336\n"
    "s_add_u32 s68, s97, 36864\n"
    "s_waitcnt vmcnt(8) lgkmcnt(0)\n"
    "buffer_load_dwordx4 v234, s[40:43], s68 offen lds\n"
    "buffer_load_dwordx4 v235, s[40:43], s68 offen offset:1024 lds\n"
    "s_add_u32 s64, s78, 24576\n"
    "s_sleep 16\n"
    "s_mov_b32 s71, 2097152\n"
    ".Le_poll:\n"
    "buffer_load_dwordx2 v[246:247], v226, s[28:31], s64 offen sc1\n"
    ".Le_chk:\n"
    "s_waitcnt vmcnt(0)\n"
    "v_cmp_ne_u64 vcc, s[56:57], v[246:247]\n"
    "s_cbranch_vccz .Le_ok\n"
    "s_sleep 16\n"
    "s_sub_u32 s71, s71, 1\n"
    "s_cbranch_scc0 .Le_poll\n"
    ".Le_ok:\n"
    "s_mov_b64 exec, 1\n"
    "ds_add_u32 v240, v241 offset:24\n"
    "s_mov_b64 exec, -1\n"
    "s_setprio 0\n"
    "s_add_u32 s69, s19, 0\n"
    "buffer_load_dwordx4 v[64:67], v242, s[28:31], s69 offen sc1\n"
    "buffer_load_dwordx4 v[68:71], v242, s[28:31], s69 offen offset:1024 sc1\n"
    "s_add_u32 s69, s19, 2048\n"
    "buffer_load_dwordx4 v[72:75], v242, s[28:31], s69 offen sc1\n"
    "buffer_load_dwordx4 v[76:79], v242, s[28:31], s69 offen offset:1024 sc1\n"
    "s_add_u32 s69, s19, 4096\n"
    "buffer_load_dwordx4 v[80:83], v242, s[28:31], s69 offen sc1\n"
    "buffer_load_dwordx4 v[84:87], v242, s[28:31], s69 offen offset:1024 sc1\n"
    "s_add_u32 s69, s19, 6144\n"
    "buffer_load_dwordx4 v[88:91], v242, s[28:31], s69 offen sc1\n"
    "buffer_load_dwordx4 v[92:95], v242, s[28:31], s69 offen offset:1024 sc1\n"
    "ds_read_b64_tr_b16 v[128:129], v232\n"
    "ds_read_b64_tr_b16 v[130:131], v232 offset:1024\n"
    "ds_read_b64_tr_b16 v[132:133], v233\n"
    "ds_read_b64_tr_b16 v[134:135], v233 offset:1024\n"
    "s_add_u32 m0, s70, 0\n"
    "s_add_u32 s68, s98, 0\n"
    "s_waitcnt vmcnt(6) lgkmcnt(0)\n"
    "v_mfma_f32_32x32x16_bf16 v[0:15], v[128:131], v[64:67], 0\n"
    "ds_read_b64_tr_b16 v[136:137], v232 offset:2048\n"
    "ds_read_b64_tr_b16 v[138:139], v232 offset:3072\n"
    "buffer_load_dwordx4 v234, s[40:43], s68 offen lds\n"
    "v_mfma_f32_32x32x16_bf16 v[32:47], v[132:135], v[64:67], 0\n"
    "ds_read_b64_tr_b16 v[140:141], v233 offset:2048\n"
    "ds_read_b64_tr_b16 v[142:143], v233 offset:3072\n"
    "buffer_load_dwordx4 v235, s[40:43], s68 offen offset:1024 lds\n"
    "v_mfma_f32_32x32x16_bf16 v[16:31], v[128:131], v[68:71], 0\n"
    "s_add_u32 s69, s20, 0\n"
    "buffer_load_dwordx4 v[96:99], v242, s[28:31], s69 offen sc1\n"
    "v_mfma_f32_32x32x16_bf16 v[48:63], v[132:135], v[68:71], 0\n"
    "buffer_load_dwordx4 v[100:103], v242, s[28:31], s69 offen offset:1024 sc1\n"
    "s_add_u32 m0, s70, 2048\n"
    "s_add_u32 s68, s98, 4096\n"
    "s_waitcnt vmcnt(6) lgkmcnt(0)\n"
    "v_mfma_f32_32x32x16_bf16 v[0:15], v[136:139], v[72:75], v[0:15]\n"
    "ds_read_b64_tr_b16 v[128:129], v232 offset:4096\n"
    "ds_read_b64_tr_b16 v[130:131], v232 offset:5120\n"
    "buffer_load_dwordx4 v234, s[40:43], s68 offen lds\n"
    "v_mfma_f32_32x32x16_bf16 v[32:47], v[140:143], v[72:75], v[32:47]\n"
    "ds_read_b64_tr_b16 v[132:133], v233 offset:4096\n"
    "ds_read_b64_tr_b16 v[134:135], v233 offset:5120\n"
    "buffer_load_dwordx4 v235, s[40:43], s68 offen offset:1024 lds\n"
    "v_mfma_f32_32x32x16_bf16 v[16:31], v[136:139], v[76:79], v[16:31]\n"
    "s_add_u32 s69, s20, 2048\n"
    "buffer_load_dwordx4 v[104:107], v242, s[28:31], s69 offen sc1\n"
    "v_mfma_f32_32x32x16_bf16 v[48:63], v[140:143], v[76:79], v[48:63]\n"
    "buffer_load_dwordx4 v[108:111], v242, s[28:31], s69 offen offset:1024 sc1\n"
    "s_add_u32 m0, s70, 4096\n"
    "s_add_u32 s68, s98, 32768\n"
    "s_waitcnt vmcnt(6) lgkmcnt(0)\n"
    "v_mfma_f32_32x32x16_bf16 v[0:15], v[128:131], v[80:83], v[0:15]\n"
    "ds_read_b64_tr_b16 v[136:137], v232 offset:6144\n"
    "ds_read_b64_tr_b16 v[138:139], v232 offset:7168\n"
    "buffer_load_dwordx4 v234, s[40:43], s68 offen lds\n"
    "v_mfma_f32_32x32x16_bf16 v[32:47], v[132:135], v[80:83], v[32:47]\n"
    "ds_read_b64_tr_b16 v[140:141], v233 offset:6144\n"
    "ds_read_b64_tr_b16 v[142:143], v233 offset:7168\n"
    "buffer_load_dwordx4 v235, s[40:43], s68 offen offset:1024 lds\n"
    "v_mfma_f32_32x32x16_bf16 v[16:31], v[128:131], v[84:87], v[16:31]\n"
    "s_add_u32 s69, s20, 4096\n"
    "buffer_load_dwordx4 v[112:115], v242, s[28:31], s69 offen sc1\n"
    "v_mfma_f32_32x32x16_bf16 v[48:63], v[132:135], v[84:87], v[48:63]\n"
    "buffer_load_dwordx4 v[116:119], v242, s[28:31], s69 offen offset:1024 sc1\n"
    "s_add_u32 m0, s70, 6144\n"
    "s_add_u32 s68, s98, 36864\n"
    "s_waitcnt vmcnt(6) lgkmcnt(0)\n"
    "v_mfma_f32_32x32x16_bf16 v[0:15], v[136:139], v[88:91], v[0:15]\n"
    "ds_read_b64_tr_b16 v[128:129], v232 offset:8192\n"
    "ds_read_b64_tr_b16 v[130:131], v232 offset:9216\n"
    "buffer_load_dwordx4 v234, s[40:43], s68 offen lds\n"
    "v_mfma_f32_32x32x16_bf16 v[32:47], v[140:143], v[88:91], v[32:47]\n"
    "ds_read_b64_tr_b16 v[132:133], v233 offset:8192\n"
    "ds_read_b64_tr_b16 v[134:135], v233 offset:9216\n"
    "buffer_load_dwordx4 v235, s[40:43], s68 offen offset:1024 lds\n"
    "v_mfma_f32_32x32x16_bf16 v[16:31], v[136:139], v[92:95], v[16:31]\n"
    "s_add_u32 s69, s20, 6144\n"
    "buffer_load_dwordx4 v[120:123], v242, s[28:31], s69 offen sc1\n"
    "v_mfma_f32_32x32x16_bf16 v[48:63], v[140:143], v[92:95], v[48:63]\n"
    "buffer_load_dwordx4 v[124:127], v242, s[28:31], s69 offen offset:1024 sc1\n"
    "s_add_u32 m0, s70, 8192\n"
    "s_add_u32 s68, s99, 0\n"
    "s_waitcnt vmcnt(6) lgkmcnt(0)\n"
    "v_mfma_f32_32x32x16_bf16 v[0:15], v[128:131], v[96:99], v[0:15]\n"
    "ds_read_b64_tr_b16 v[136:137], v232 offset:10240\n"
    "ds_read_b64_tr_b16 v[138:139], v232 offset:11264\n"
    "buffer_load_dwordx4 v234, s[40:43], s68 offen lds\n"
    "v_mfma_f32_32x32x16_bf16 v[32:47], v[132:135], v[96:99], v[32:47]\n"
    "ds_read_b64_tr_b16 v[140:141], v233 offset:10240\n"
    "ds_read_b64_tr_b16 v[142:143], v233 offset:11264\n"
    "buffer_load_dwordx4 v235, s[40:43], s68 offen offset:1024 lds\n"
    "v_mfma_f32_32x32x16_bf16 v[16:31], v[128:131], v[100:103], v[16:31]\n"
    "s_add_u32 s69, s21, 0\n"
    "buffer_load_dwordx4 v[64:67], v242, s[28:31], s69 offen sc1\n"
    "v_mfma_f32_32x32x16_bf16 v[48:63], v[132:135], v[100:103], v[48:63]\n"
    "buffer_load_dwordx4 v[68:71], v242, s[28:31], s69 offen offset:1024 sc1\n"
    "s_add_u32 m0, s70, 10240\n"
    "s_add_u32 s68, s99, 4096\n"
    "s_waitcnt vmcnt(6) lgkmcnt(0)\n"
    "v_mfma_f32_32x32x16_bf16 v[0:15], v[136:139], v[104:107], v[0:15]\n"
    "ds_read_b64_tr_b16 v[128:129], v232 offset:12288\n"
    "ds_read_b64_tr_b16 v[130:131], v232 offset:13312\n"
    "buffer_load_dwordx4 v234, s[40:43], s68 offen lds\n"
    "v_mfma_f32_32x32x16_bf16 v[32:47], v[140:143], v[104:107], v[32:47]\n"
    "ds_read_b64_tr_b16 v[132:133], v233 offset:12288\n"
    "ds_read_b64_tr_b16 v[134:135], v233 offset:13312\n"
    "buffer_load_dwordx4 v235, s[40:43], s68 offen offset:1024 lds\n"
    "v_mfma_f32_32x32x16_bf16 v[16:31], v[136:139], v[108:111], v[16:31]\n"
    "s_add_u32 s69, s21, 2048\n"
    "buffer_load_dwordx4 v[72:75], v242, s[28:31], s69 offen sc1\n"
    "v_mfma_f32_32x32x16_bf16 v[48:63], v[140:143], v[108:111], v[48:63]\n"
    "buffer_load_dwordx4 v[76:79], v242, s[28:31], s69 offen offset:1024 sc1\n"
    "s_add_u32 m0, s70, 12288\n"
    "s_add_u32 s68, s99, 32768\n"
    "s_waitcnt vmcnt(6) lgkmcnt(0)\n"
    "v_mfma_f32_32x32x16_bf16 v[0:15], v[128:131], v[112:115], v[0:15]\n"
    "ds_read_b64_tr_b16 v[136:137], v232 offset:14336\n"
    "ds_read_b64_tr_b16 v[138:139], v232 offset:15360\n"
    "buffer_load_dwordx4 v234, s[40:43], s68 offen lds\n"
    "v_mfma_f32_32x32x16_bf16 v[32:47], v[132:135], v[112:115], v[32:47]\n"
    "ds_read_b64_tr_b16 v[140:141], v233 offset:14336\n"
    "ds_read_b64_tr_b16 v[142:143], v233 offset:15360\n"
    "buffer_load_dwordx4 v235, s[40:43], s68 offen offset:1024 lds\n"
    "v_mfma_f32_32x32x16_bf16 v[16:31], v[128:131], v[116:119], v[16:31]\n"
    "s_add_u32 s69, s21, 4096\n"
    "buffer_load_dwordx4 v[80:83], v242, s[28:31], s69 offen sc1\n"
    "v_mfma_f32_32x32x16_bf16 v[48:63], v[132:135], v[116:119], v[48:63]\n"
    "buffer_load_dwordx4 v[84:87], v242, s[28:31], s69 offen offset:1024 sc1\n"
    "s_add_u32 m0, s70, 14336\n"
    "s_add_u32 s68, s99, 36864\n"
    "s_waitcnt vmcnt(6) lgkmcnt(0)\n"
    "v_mfma_f32_32x32x16_bf16 v[0:15], v[136:139], v[120:123], v[0:15]\n"
    "ds_read_b64_tr_b16 v[128:129], v232\n"
    "ds_read_b64_tr_b16 v[130:131], v232 offset:1024\n"
    "buffer_load_dwordx4 v234, s[40:43], s68 offen lds\n"
    "v_mfma_f32_32x32x16_bf16 v[32:47], v[140:143], v[120:123], v[32:47]\n"
    "ds_read_b64_tr_b16 v[132:133], v233\n"
    "ds_read_b64_tr_b16 v[134:135], v233 offset:1024\n"
    "buffer_load_dwordx4 v235, s[40:43], s68 offen offset:1024 lds\n"
    "v_mfma_f32_32x32x16_bf16 v[16:31], v[136:139], v[124:127], v[16:31]\n"
    "s_add_u32 s69, s21, 6144\n"
    "buffer_load_dwordx4 v[88:91], v242, s[28:31], s69 offen sc1\n"
    "v_mfma_f32_32x32x16_bf16 v[48:63], v[140:143], v[124:127], v[48:63]\n"
    "buffer_load_dwordx4 v[92:95], v242, s[28:31], s69 offen offset:1024 sc1\n"
    "s_add_u32 m0, s70, 0\n"
    "s_add_u32 s68, s4, 0\n"
    "s_waitcnt vmcnt(6) lgkmcnt(0)\n"
    "v_mfma_f32_32x32x16_bf16 v[0:15], v[128:131], v[64:67], v[0:15]\n"
    "ds_read_b64_tr_b16 v[136:137], v232 offset:2048\n"
    "ds_read_b64_tr_b16 v[138:139], v232 offset:3072\n"
    "buffer_load_dwordx4 v234, s[40:43], s68 offen lds\n"
    "v_mfma_f32_32x32x16_bf16 v[32:47], v[132:135], v[64:67], v[32:47]\n"
    "ds_read_b64_tr_b16 v[140:141], v233 offset:2048\n"
    "ds_read_b64_tr_b16 v[142:143], v233 offset:3072\n"
    "buffer_load_dwordx4 v235, s[40:43], s68 offen offset:1024 lds\n"
    "v_mfma_f32_32x32x16_bf16 v[16:31], v[128:131], v[68:71], v[16:31]\n"
    "s_add_u32 s69, s22, 0\n"
    "buffer_load_dwordx4 v[96:99], v242, s[28:31], s69 offen sc1\n"
    "v_mfma_f32_32x32x16_bf16 v[48:63], v[132:135], v[68:71], v[48:63]\n"
    "buffer_load_dwordx4 v[100:103], v242, s[28:31], s69 offen offset:1024 sc1\n"
    "s_add_u32 m0, s70, 2048\n"
    "s_add_u32 s68, s4, s62\n"
    "s_waitcnt vmcnt(6) lgkmcnt(0)\n"
    "v_mfma_f32_32x32x16_bf16 v[0:15], v[136:139], v[72:75], v[0:15]\n"
    "ds_read_b64_tr_b16 v[128:129], v232 offset:4096\n"
    "ds_read_b64_tr_b16 v[130:131], v232 offset:5120\n"
    "buffer_load_dwordx4 v234, s[40:43], s68 offen lds\n"
    "v_mfma_f32_32x32x16_bf16 v[32:47], v[140:143], v[72:75], v[32:47]\n"
    "ds_read_b64_tr_b16 v[132:133], v233 offset:4096\n"
    "ds_read_b64_tr_b16 v[134:135], v233 offset:5120\n"
    "buffer_load_dwordx4 v235, s[40:43], s68 offen offset:1024 lds\n"
    "v_mfma_f32_32x32x16_bf16 v[16:31], v[136:139], v[76:79], v[16:31]\n"
    "s_add_u32 s69, s22, 2048\n"
    "buffer_load_dwordx4 v[104:107], v242, s[28:31], s69 offen sc1\n"
    "v_mfma_f32_32x32x16_bf16 v[48:63], v[140:143], v[76:79], v[48:63]\n"
    "buffer_load_dwordx4 v[108:111], v242, s[28:31], s69 offen offset:1024 sc1\n"
    "s_add_u32 m0, s70, 4096\n"
    "s_add_u32 s68, s4, 32768\n"
    "s_waitcnt vmcnt(6) lgkmcnt(0)\n"
    "v_mfma_f32_32x32x16_bf16 v[0:15], v[128:131], v[80:83], v[0:15]\n"
    "ds_read_b64_tr_b16 v[136:137], v232 offset:6144\n"
    "ds_read_b64_tr_b16 v[138:139], v232 offset:7168\n"
    "buffer_load_dwordx4 v234, s[40:43], s68 offen lds\n"
    "v_mfma_f32_32x32x16_bf16 v[32:47], v[132:135], v[80:83], v[32:47]\n"
    "ds_read_b64_tr_b16 v[140:141], v233 offset:6144\n"
    "ds_read_b64_tr_b16 v[142:143], v233 offset:7168\n"
    "buffer_load_dwordx4 v235, s[40:43], s68 offen offset:1024 lds\n"
    "v_mfma_f32_32x32x16_bf16 v[16:31], v[128:131], v[84:87], v[16:31]\n"
    "s_add_u32 s69, s22, 4096\n"
    "buffer_load_dwordx4 v[112:115], v242, s[28:31], s69 offen sc1\n"
    "v_mfma_f32_32x32x16_bf16 v[48:63], v[132:135], v[84:87], v[48:63]\n"
    "buffer_load_dwordx4 v[116:119], v242, s[28:31], s69 offen offset:1024 sc1\n"
    "s_add_u32 m0, s70, 6144\n"
    "s_add_u32 s68, s5, 0\n"
    "s_waitcnt vmcnt(6) lgkmcnt(0)\n"
    "v_mfma_f32_32x32x16_bf16 v[0:15], v[136:139], v[88:91], v[0:15]\n"
    "ds_read_b64_tr_b16 v[128:129], v232 offset:8192\n"
    "ds_read_b64_tr_b16 v[130:131], v232 offset:9216\n"
    "buffer_load_dwordx4 v234, s[40:43], s68 offen lds\n"
    "v_mfma_f32_32x32x16_bf16 v[32:47], v[140:143], v[88:91], v[32:47]\n"
    "ds_read_b64_tr_b16 v[132:133], v233 offset:8192\n"
    "ds_read_b64_tr_b16 v[134:135], v233 offset:9216\n"
    "buffer_load_dwordx4 v235, s[40:43], s68 offen offset:1024 lds\n"
    "v_mfma_f32_32x32x16_bf16 v[16:31], v[136:139], v[92:95], v[16:31]\n"
    "s_add_u32 s69, s22, 6144\n"
    "buffer_load_dwordx4 v[120:123], v242, s[28:31], s69 offen sc1\n"
    "v_mfma_f32_32x32x16_bf16 v[48:63], v[140:143], v[92:95], v[48:63]\n"
    "buffer_load_dwordx4 v[124:127], v242, s[28:31], s69 offen offset:1024 sc1\n"
    "s_add_u32 m0, s70, 8192\n"
    "s_add_u32 s68, s5, s62\n"
    "s_waitcnt vmcnt(8) lgkmcnt(0)\n"
    "v_mfma_f32_32x32x16_bf16 v[0:15], v[128:131], v[96:99], v[0:15]\n"
    "ds_read_b64_tr_b16 v[136:137], v232 offset:10240\n"
    "ds_read_b64_tr_b16 v[138:139], v232 offset:11264\n"
    "buffer_load_dwordx4 v234, s[40:43], s68 offen lds\n"
    "v_mfma_f32_32x32x16_bf16 v[32:47], v[132:135], v[96:99], v[32:47]\n"
    "ds_read_b64_tr_b16 v[140:141], v233 offset:10240\n"
    "ds_read_b64_tr_b16 v[142:143], v233 offset:11264\n"
    "buffer_load_dwordx4 v235, s[40:43], s68 offen offset:1024 lds\n"
    "v_mfma_f32_32x32x16_bf16 v[16:31], v[128:131], v[100:103], v[16:31]\n"
    "v_mfma_f32_32x32x16_bf16 v[48:63], v[132:135], v[100:103], v[48:63]\n"
    "s_add_u32 m0, s70, 10240\n"
    "s_add_u32 s68, s5, 32768\n"
    "s_waitcnt vmcnt(8) lgkmcnt(0)\n"
    "v_mfma_f32_32x32x16_bf16 v[0:15], v[136:139], v[104:107], v[0:15]\n"
    "ds_read_b64_tr_b16 v[128:129], v232 offset:12288\n"
    "ds_read_b64_tr_b16 v[130:131], v232 offset:13312\n"
    "buffer_load_dwordx4 v234, s[40:43], s68 offen lds\n"
    "v_mfma_f32_32x32x16_bf16 v[32:47], v[140:143], v[104:107], v[32:47]\n"
    "ds_read_b64_tr_b16 v[132:133], v233 offset:12288\n"
    "ds_read_b64_tr_b16 v[134:135], v233 offset:13312\n"
    "buffer_load_dwordx4 v235, s[40:43], s68 offen offset:1024 lds\n"
    "v_mfma_f32_32x32x16_bf16 v[16:31], v[136:139], v[108:111], v[16:31]\n"
    "v_mfma_f32_32x32x16_bf16 v[48:63], v[140:143], v[108:111], v[48:63]\n"
    "s_add_u32 m0, s70, 12288\n"
    "s_add_u32 s68, s6, 0\n"
    "s_waitcnt vmcnt(8) lgkmcnt(0)\n"
    "v_mfma_f32_32x32x16_bf16 v[0:15], v[128:131], v[112:115], v[0:15]\n"
    "ds_read_b64_tr_b16 v[136:137], v232 offset:14336\n"
    "ds_read_b64_tr_b16 v[138:139], v232 offset:15360\n"
    "buffer_load_dwordx4 v234, s[40:43], s68 offen lds\n"
    "v_mfma_f32_32x32x16_bf16 v[32:47], v[132:135], v[112:115], v[32:47]\n"
    "ds_read_b64_tr_b16 v[140:141], v233 offset:14336\n"
    "ds_read_b64_tr_b16 v[142:143], v233 offset:15360\n"
    "buffer_load_dwordx4 v235, s[40:43], s68 offen offset:1024 lds\n"
    "v_mfma_f32_32x32x16_bf16 v[16:31], v[128:131], v[116:119], v[16:31]\n"
    "v_mfma_f32_32x32x16_bf16 v[48:63], v[132:135], v[116:119], v[48:63]\n"
    "s_add_u32 m0, s70, 14336\n"
    "s_add_u32 s68, s6, s62\n"
    "s_waitcnt vmcnt(6) lgkmcnt(0)\n"
    "v_mfma_f32_32x32x16_bf16 v[0:15], v[136:139], v[120:123], v[0:15]\n"
    "buffer_load_dwordx4 v234, s[40:43], s68 offen lds\n"
    "v_mfma_f32_32x32x16_bf16 v[32:47], v[140:143], v[120:123], v[32:47]\n"
    "buffer_load_dwordx4 v235, s[40:43], s68 offen offset:1024 lds\n"
    "v_mfma_f32_32x32x16_bf16 v[16:31], v[136:139], v[124:127], v[16:31]\n"
    "v_mfma_f32_32x32x16_bf16 v[48:63], v[140:143], v[124:127], v[48:63]\n"
    "s_add_u32 m0, s76, 0\n"
    "s_add_u32 s68, s6, 32768\n"
    "s_waitcnt vmcnt(8)\n"
    "buffer_load_dwordx4 v234, s[40:43], s68 offen lds\n"
    "buffer_load_dwordx4 v235, s[40:43], s68 offen offset:1024 lds\n"
    "s_add_u32 m0, s76, 2048\n"
    "s_add_u32 s68, s7, 0\n"
    "s_waitcnt vmcnt(8)\n"
    "buffer_load_dwordx4 v234, s[40:43], s68 offen lds\n"
    "buffer_load_dwordx4 v235, s[40:43], s68 offen offset:1024 lds\n"
    "s_add_u32 m0, s76, 4096\n"
    "s_add_u32 s68, s7, s62\n"
    "s_waitcnt vmcnt(8)\n"
    "buffer_load_dwordx4 v234, s[40:43], s68 offen lds\n"
    "buffer_load_dwordx4 v235, s[40:43], s68 offen offset:1024 lds\n"
    "s_setprio 1\n"
    "s_mov_b32 s77, 0\n"
    ".Lg2:\n"
    "s_waitcnt vmcnt(6)\n"
    "ds_read_b64_tr_b16 v[128:129], v232\n"
    "ds_read_b64_tr_b16 v[130:131], v232 offset:1024\n"
    "ds_read_b64_tr_b16 v[132:133], v233\n"
    "ds_read_b64_tr_b16 v[134:135], v233 offset:1024\n"
    "ds_read_b64_tr_b16 v[136:137], v232 offset:2048\n"
    "ds_read_b64_tr_b16 v[138:139], v232 offset:3072\n"
    "ds_read_b64_tr_b16 v[140:141], v233 offset:2048\n"
    "ds_read_b64_tr_b16 v[142:143], v233 offset:3072\n"
    "s_add_u32 m0, s70, 0\n"
    "s_add_u32 s68, s7, 32768\n"
    "s_waitcnt vmcnt(62) lgkmcnt(4)\n"
    "buffer_load_dwordx4 v234, s[40:43], s68 offen lds\n"
    "buffer_load_dwordx4 v235, s[40:43], s68 offen offset:1024 lds\n"
    "ds_read_b64_tr_b16 v[144:145], v232 offset:4096\n"
    "ds_read_b64_tr_b16 v[146:147], v232 offset:5120\n"
    "ds_read_b64_tr_b16 v[148:149], v233 offset:4096\n"
    "ds_read_b64_tr_b16 v[150:151], v233 offset:5120\n"
    "s_add_u32 m0, s70, 2048\n"
    "s_add_u32 s68, s8, 0\n"
    "s_waitcnt vmcnt(62) lgkmcnt(4)\n"
    "buffer_load_dwordx4 v234, s[40:43], s68 offen lds\n"
    "buffer_load_dwordx4 v235, s[40:43], s68 offen offset:1024 lds\n"
    "ds_read_b64_tr_b16 v[152:153], v232 offset:6144\n"
    "ds_read_b64_tr_b16 v[154:155], v232 offset:7168\n"
    "ds_read_b64_tr_b16 v[156:157], v233 offset:6144\n"
    "ds_read_b64_tr_b16 v[158:159], v233 offset:7168\n"
    "s_add_u32 m0, s70, 4096\n"
    "s_add_u32 s68, s8, s62\n"
    "s_waitcnt vmcnt(62) lgkmcnt(4)\n"
    "buffer_load_dwordx4 v234, s[40:43], s68 offen lds\n"
    "buffer_load_dwordx4 v235, s[40:43], s68 offen offset:1024 lds\n"
    "s_add_u32 m0, s70, 6144\n"
    "s_add_u32 s68, s8, 32768\n"
    "s_waitcnt vmcnt(62) lgkmcnt(0)\n"
    "buffer_load_dwordx4 v234, s[40:43], s68 offen lds\n"
    "buffer_load_dwordx4 v235, s[40:43], s68 offen offset:1024 lds\n"
    "v_and_b32 v247, 7, v236\n"
    "v_lshlrev_b32 v248, 8, v236\n"
    "v_add_u32 v248, s70, v248\n"
    "v_add_u32 v246, 0, v237\n"
    "v_xor_b32 v246, v246, v247\n"
    "v_lshl_add_u32 v228, v246, 4, v248\n"
    "v_add_u32 v246, 2, v237\n"
    "v_xor_b32 v246, v246, v247\n"
    "v_lshl_add_u32 v229, v246, 4, v248\n"
    "v_add_u32 v246, 4, v237\n"
    "v_xor_b32 v246, v246, v247\n"
    "v_lshl_add_u32 v230, v246, 4, v248\n"
    "v_add_u32 v246, 6, v237\n"
    "v_xor_b32 v246, v246, v247\n"
    "v_lshl_add_u32 v231, v246, 4, v248\n"
    "v_add_u32 v251, 4096, v242\n"
    "s_add_u32 s58, s23, 0\n"
    "s_add_u32 s59, s23, 4096\n"
    "s_add_u32 s60, s23, 8192\n"
    "s_add_u32 s61, s23, 12288\n"
    "s_mov_b64 s[32:33], s[28:29]\n"
    "s_mov_b32 s34, 0\n"
    "s_mov_b64 s[36:37], s[28:29]\n"
    "s_mov_b32 s38, 0\n"
    "s_cmp_lg_u32 s77, 0\n"
    "s_cbranch_scc1 .Lh_run\n"
    "s_add_u32 s64, s78, 32768\n"
    "ds_read_b32 v246, v240 offset:32\n"
    "s_waitcnt lgkmcnt(0)\n"
    "v_cmp_gt_u32 vcc, 3, v246\n"
    "s_cbranch_vccz .Lh_start\n"
    "s_mov_b32 s71, 8388608\n"
    ".Lhd5135_spin:\n"
    "ds_read_b32 v246, v240 offset:32\n"
    "s_waitcnt lgkmcnt(0)\n"
    "v_cmp_gt_u32 vcc, 3, v246\n"
    "s_cbranch_vccz .Lhd5135_go\n"
    "s_sleep 4\n"
    "s_sub_u32 s71, s71, 1\n"
    "s_cbranch_scc0 .Lhd5135_spin\n"
    ".Lhd5135_go:\n"
    "s_sleep 8\n"
    ".Lh_start:\n"
    "s_mov_b32 s71, 2097152\n"
    ".Lh_poll:\n"
    "buffer_load_dwordx2 v[246:247], v226, s[28:31], s64 offen sc1\n"
    ".Lh_chk:\n"
    "s_waitcnt vmcnt(0)\n"
    "v_cmp_ne_u64 vcc, s[56:57], v[246:247]\n"
    "s_cbranch_vccz .Lh_ok\n"
    "s_sleep 4\n"
    "s_sub_u32 s71, s71, 1\n"
    "s_cbranch_scc0 .Lh_poll\n"
    ".Lh_ok:\n"
    "buffer_load_dwordx4 v[64:67], v242, s[28:31], s12 offen sc1\n"
    "buffer_load_dwordx4 v[68:71], v242, s[28:31], s12 offen offset:1024 sc1\n"
    "buffer_load_dwordx4 v[72:75], v242, s[28:31], s12 offen offset:2048 sc1\n"
    "buffer_load_dwordx4 v[76:79], v242, s[28:31], s12 offen offset:3072 sc1\n"
    "buffer_load_dwordx4 v[80:83], v251, s[28:31], s12 offen sc1\n"
    "buffer_load_dwordx4 v[84:87], v251, s[28:31], s12 offen offset:1024 sc1\n"
    "buffer_load_dwordx4 v[88:91], v242, s[28:31], s13 offen sc1\n"
    "buffer_load_dwordx4 v[92:95], v242, s[28:31], s13 offen offset:1024 sc1\n"
    ".Lh_run:\n"
    "s_waitcnt vmcnt(6) lgkmcnt(4)\n"
    "s_setprio 0\n"
    "v_mfma_f32_32x32x16_bf16 v[0:15], v[128:131], v[64:67], v[0:15]\n"
    "buffer_load_dwordx4 v[96:99], v242, s[28:31], s13 offen offset:2048 sc1\n"
    "v_mfma_f32_32x32x16_bf16 v[32:47], v[132:135], v[64:67], v[32:47]\n"
    "buffer_load_dwordx4 v[100:103], v242, s[28:31], s13 offen offset:3072 sc1\n"
    "v_mfma_f32_32x32x16_bf16 v[16:31], v[128:131], v[68:71], v[16:31]\n"
    "buffer_load_dwordx4 v[104:107], v251, s[28:31], s13 offen sc1\n"
    "v_mfma_f32_32x32x16_bf16 v[48:63], v[132:135], v[68:71], v[48:63]\n"
    "buffer_load_dwordx4 v[108:111], v251, s[28:31], s13 offen offset:1024 sc1\n"
    "s_waitcnt vmcnt(8) lgkmcnt(4)\n"
    "v_mfma_f32_32x32x16_bf16 v[0:15], v[136:139], v[72:75], v[0:15]\n"
    "ds_read_b64_tr_b16 v[128:129], v232 offset:8192\n"
    "buffer_load_dwordx4 v[112:115], v242, s[28:31], s14 offen sc1\n"
    "v_mfma_f32_32x32x16_bf16 v[32:47], v[140:143], v[72:75], v[32:47]\n"
    "ds_read_b64_tr_b16 v[130:131], v232 offset:9216\n"
    "buffer_load_dwordx4 v[116:119], v242, s[28:31], s14 offen offset:1024 sc1\n"
    "v_mfma_f32_32x32x16_bf16 v[16:31], v[136:139], v[76:79], v[16:31]\n"
    "ds_read_b64_tr_b16 v[132:133], v233 offset:8192\n"
    "buffer_load_dwordx4 v[120:123], v242, s[28:31], s14 offen offset:2048 sc1\n"
    "v_mfma_f32_32x32x16_bf16 v[48:63], v[140:143], v[76:79], v[48:63]\n"
    "ds_read_b64_tr_b16 v[134:135], v233 offset:9216\n"
    "buffer_load_dwordx4 v[124:127], v242, s[28:31], s14 offen offset:3072 sc1\n"
    "s_waitcnt vmcnt(10) lgkmcnt(4)\n"
    "v_mfma_f32_32x32x16_bf16 v[0:15], v[144:147], v[80:83], v[0:15]\n"
    "ds_read_b64_tr_b16 v[136:137], v232 offset:10240\n"
    "buffer_load_dwordx4 v[64:67], v251, s[28:31], s14 offen sc1\n"
    "v_mfma_f32_32x32x16_bf16 v[32:47], v[148:151], v[80:83], v[32:47]\n"
    "ds_read_b64_tr_b16 v[138:139], v232 offset:11264\n"
    "buffer_load_dwordx4 v[68:71], v251, s[28:31], s14 offen offset:1024 sc1\n"
    "v_mfma_f32_32x32x16_bf16 v[16:31], v[144:147], v[84:87], v[16:31]\n"
    "ds_read_b64_tr_b16 v[140:141], v233 offset:10240\n"
    "buffer_load_dwordx4 v[72:75], v242, s[28:31], s15 offen sc1\n"
    "v_mfma_f32_32x32x16_bf16 v[48:63], v[148:151], v[84:87], v[48:63]\n"
    "ds_read_b64_tr_b16 v[142:143], v233 offset:11264\n"
    "buffer_load_dwordx4 v[76:79], v242, s[28:31], s15 offen offset:1024 sc1\n"
    "s_add_u32 m0, s70, 8192\n"
    "s_add_u32 s68, s9, 0\n"
    "s_waitcnt vmcnt(12) lgkmcnt(4)\n"
    "v_mfma_f32_32x32x16_bf16 v[0:15], v[152:155], v[88:91], v[0:15]\n"
    "ds_read_b64_tr_b16 v[144:145], v232 offset:12288\n"
    "buffer_load_dwordx4 v234, s[40:43], s68 offen lds\n"
    "v_mfma_f32_32x32x16_bf16 v[32:47], v[156:159], v[88:91], v[32:47]\n"
    "ds_read_b64_tr_b16 v[146:147], v232 offset:13312\n"
    "buffer_load_dwordx4 v235, s[40:43], s68 offen offset:1024 lds\n"
    "v_mfma_f32_32x32x16_bf16 v[16:31], v[152:155], v[92:95], v[16:31]\n"
    "ds_read_b64_tr_b16 v[148:149], v233 offset:12288\n"
    "buffer_load_dwordx4 v[80:83], v242, s[28:31], s15 offen offset:2048 sc1\n"
    "v_mfma_f32_32x32x16_bf16 v[48:63], v[156:159], v[92:95], v[48:63]\n"
    "ds_read_b64_tr_b16 v[150:151], v233 offset:13312\n"
    "buffer_load_dwordx4 v[84:87], v242, s[28:31], s15 offen offset:3072 sc1\n"
    "s_add_u32 m0, s70, 10240\n"
    "s_add_u32 s68, s9, s62\n"
    "s_waitcnt vmcnt(14) lgkmcnt(4)\n"
    "v_mfma_f32_32x32x16_bf16 v[0:15], v[128:131], v[96:99], v[0:15]\n"
    "ds_read_b64_tr_b16 v[152:153], v232 offset:14336\n"
    "buffer_load_dwordx4 v234, s[40:43], s68 offen lds\n"
    "v_mfma_f32_32x32x16_bf16 v[32:47], v[132:135], v[96:99], v[32:47]\n"
    "ds_read_b64_tr_b16 v[154:155], v232 offset:15360\n"
    "buffer_load_dwordx4 v235, s[40:43], s68 offen offset:1024 lds\n"
    "v_mfma_f32_32x32x16_bf16 v[16:31], v[128:131], v[100:103], v[16:31]\n"
    "ds_read_b64_tr_b16 v[156:157], v233 offset:14336\n"
    "buffer_load_dwordx4 v[88:91], v251, s[28:31], s15 offen sc1\n"
    "v_mfma_f32_32x32x16_bf16 v[48:63], v[132:135], v[100:103], v[48:63]\n"
    "ds_read_b64_tr_b16 v[158:159], v233 offset:15360\n"
    "buffer_load_dwordx4 v[92:95], v251, s[28:31], s15 offen offset:1024 sc1\n"
    "s_add_u32 m0, s70, 12288\n"
    "s_add_u32 s68, s9, 32768\n"
    "s_waitcnt vmcnt(16) lgkmcnt(4)\n"
    "v_mfma_f32_32x32x16_bf16 v[0:15], v[136:139], v[104:107], v[0:15]\n"
    "ds_read_b64_tr_b16 v[128:129], v238\n"
    "buffer_load_dwordx4 v234, s[40:43], s68 offen lds\n"
    "v_mfma_f32_32x32x16_bf16 v[32:47], v[140:143], v[104:107], v[32:47]\n"
    "ds_read_b64_tr_b16 v[130:131], v238 offset:1024\n"
    "buffer_load_dwordx4 v235, s[40:43], s68 offen offset:1024 lds\n"
    "v_mfma_f32_32x32x16_bf16 v[16:31], v[136:139], v[108:111], v[16:31]\n"
    "ds_read_b64_tr_b16 v[132:133], v239\n"
    "buffer_load_dwordx4 v[96:99], v242, s[28:31], s16 offen sc1\n"
    "v_mfma_f32_32x32x16_bf16 v[48:63], v[140:143], v[108:111], v[48:63]\n"
    "ds_read_b64_tr_b16 v[134:135], v239 offset:1024\n"
    "buffer_load_dwordx4 v[100:103], v242, s[28:31], s16 offen offset:1024 sc1\n"
    "s_add_u32 m0, s70, 14336\n"
    "s_add_u32 s68, s10, 0\n"
    "s_waitcnt vmcnt(18) lgkmcnt(4)\n"
    "v_mfma_f32_32x32x16_bf16 v[0:15], v[144:147], v[112:115], v[0:15]\n"
    "ds_read_b64_tr_b16 v[136:137], v238 offset:2048\n"
    "buffer_load_dwordx4 v234, s[40:43], s68 offen lds\n"
    "v_mfma_f32_32x32x16_bf16 v[32:47], v[148:151], v[112:115], v[32:47]\n"
    "ds_read_b64_tr_b16 v[138:139], v238 offset:3072\n"
    "buffer_load_dwordx4 v235, s[40:43], s68 offen offset:1024 lds\n"
    "v_mfma_f32_32x32x16_bf16 v[16:31], v[144:147], v[116:119], v[16:31]\n"
    "ds_read_b64_tr_b16 v[140:141], v239 offset:2048\n"
    "buffer_load_dwordx4 v[104:107], v242, s[28:31], s16 offen offset:2048 sc1\n"
    "v_mfma_f32_32x32x16_bf16 v[48:63], v[148:151], v[116:119], v[48:63]\n"
    "ds_read_b64_tr_b16 v[142:143], v239 offset:3072\n"
    "buffer_load_dwordx4 v[108:111], v242, s[28:31], s16 offen offset:3072 sc1\n"
    "s_add_u32 m0, s76, 0\n"
    "s_add_u32 s68, s10, s62\n"
    "s_waitcnt vmcnt(20) lgkmcnt(4)\n"
    "v_mfma_f32_32x32x16_bf16 v[0:15], v[152:155], v[120:123], v[0:15]\n"
    "ds_read_b64_tr_b16 v[144:145], v238 offset:4096\n"
    "buffer_load_dwordx4 v234, s[40:43], s68 offen lds\n"
    "v_mfma_f32_32x32x16_bf16 v[32:47], v[156:159], v[120:123], v[32:47]\n"
    "ds_read_b64_tr_b16 v[146:147], v238 offset:5120\n"
    "buffer_load_dwordx4 v235, s[40:43], s68 offen offset:1024 lds\n"
    "v_mfma_f32_32x32x16_bf16 v[16:31], v[152:155], v[124:127], v[16:31]\n"
    "ds_read_b64_tr_b16 v[148:149], v239 offset:4096\n"
    "buffer_load_dwordx4 v[112:115], v251, s[28:31], s16 offen sc1\n"
    "v_mfma_f32_32x32x16_bf16 v[48:63], v[156:159], v[124:127], v[48:63]\n"
    "ds_read_b64_tr_b16 v[150:151], v239 offset:5120\n"
    "buffer_load_dwordx4 v[116:119], v251, s[28:31], s16 offen offset:1024 sc1\n"
    "s_add_u32 m0, s76, 2048\n"
    "s_add_u32 s68, s10, 32768\n"
    "s_waitcnt vmcnt(22) lgkmcnt(4)\n"
    "v_mfma_f32_32x32x16_bf16 v[0:15], v[128:131], v[64:67], v[0:15]\n"
    "ds_read_b64_tr_b16 v[152:153], v232\n"
    "buffer_load_dwordx4 v234, s[40:43], s68 offen lds\n"
    "v_mfma_f32_32x32x16_bf16 v[32:47], v[132:135], v[64:67], v[32:47]\n"
    "ds_read_b64_tr_b16 v[154:155], v232 offset:1024\n"
    "buffer_load_dwordx4 v235, s[40:43], s68 offen offset:1024 lds\n"
    "v_mfma_f32_32x32x16_bf16 v[16:31], v[128:131], v[68:71], v[16:31]\n"
    "ds_read_b64_tr_b16 v[156:157], v233\n"
    "buffer_load_dwordx4 v[120:123], v242, s[28:31], s17 offen sc1\n"
    "v_mfma_f32_32x32x16_bf16 v[48:63], v[132:135], v[68:71], v[48:63]\n"
    "ds_read_b64_tr_b16 v[158:159], v233 offset:1024\n"
    "buffer_load_dwordx4 v[124:127], v242, s[28:31], s17 offen offset:1024 sc1\n"
    "s_waitcnt vmcnt(24) lgkmcnt(4)\n"
    "v_mfma_f32_32x32x16_bf16 v[0:15], v[136:139], v[72:75], v[0:15]\n"
    "ds_read_b64_tr_b16 v[128:129], v232 offset:2048\n"
    "buffer_load_dwordx4 v[64:67], v242, s[28:31], s17 offen offset:2048 sc1\n"
    "v_mfma_f32_32x32x16_bf16 v[32:47], v[140:143], v[72:75], v[32:47]\n"
    "ds_read_b64_tr_b16 v[130:131], v232 offset:3072\n"
    "buffer_load_dwordx4 v[68:71], v242, s[28:31], s17 offen offset:3072 sc1\n"
    "v_mfma_f32_32x32x16_bf16 v[16:31], v[136:139], v[76:79], v[16:31]\n"
    "ds_read_b64_tr_b16 v[132:133], v233 offset:2048\n"
    "buffer_load_dwordx4 v[72:75], v251, s[28:31], s17 offen sc1\n"
    "v_mfma_f32_32x32x16_bf16 v[48:63], v[140:143], v[76:79], v[48:63]\n"
    "ds_read_b64_tr_b16 v[134:135], v233 offset:3072\n"
    "buffer_load_dwordx4 v[76:79], v251, s[28:31], s17 offen offset:1024 sc1\n"
    "s_waitcnt vmcnt(24) lgkmcnt(4)\n"
    "v_mfma_f32_32x32x16_bf16 v[0:15], v[144:147], v[80:83], v[0:15]\n"
    "ds_read_b64_tr_b16 v[136:137], v232 offset:4096\n"
    "v_mfma_f32_32x32x16_bf16 v[32:47], v[148:151], v[80:83], v[32:47]\n"
    "ds_read_b64_tr_b16 v[138:139], v232 offset:5120\n"
    "v_mfma_f32_32x32x16_bf16 v[16:31], v[144:147], v[84:87], v[16:31]\n"
    "ds_read_b64_tr_b16 v[140:141], v233 offset:4096\n"
    "buffer_load_dwordx4 v[80:83], v242, s[28:31], s18 offen sc1\n"
    "v_mfma_f32_32x32x16_bf16 v[48:63], v[148:151], v[84:87], v[48:63]\n"
    "ds_read_b64_tr_b16 v[142:143], v233 offset:5120\n"
    "buffer_load_dwordx4 v[84:87], v242, s[28:31], s18 offen offset:1024 sc1\n"
    "s_waitcnt vmcnt(22) lgkmcnt(4)\n"
    "ds_read_b32 v246, v240 offset:16\n"
    "v_mfma_f32_32x32x16_bf16 v[0:15], v[152:155], v[88:91], v[0:15]\n"
    "ds_read_b64_tr_b16 v[144:145], v232 offset:6144\n"
    "v_mfma_f32_32x32x16_bf16 v[32:47], v[156:159], v[88:91], v[32:47]\n"
    "ds_read_b64_tr_b16 v[146:147], v232 offset:7168\n"
    "v_mfma_f32_32x32x16_bf16 v[16:31], v[152:155], v[92:95], v[16:31]\n"
    "ds_read_b64_tr_b16 v[148:149], v233 offset:6144\n"
    "buffer_load_dwordx4 v[88:91], v242, s[28:31], s18 offen offset:2048 sc1\n"
    "v_mfma_f32_32x32x16_bf16 v[48:63], v[156:159], v[92:95], v[48:63]\n"
    "ds_read_b64_tr_b16 v[150:151], v233 offset:7168\n"
    "buffer_load_dwordx4 v[92:95], v242, s[28:31], s18 offen offset:3072 sc1\n"
    "s_waitcnt vmcnt(20) lgkmcnt(4)\n"
    "s_setprio 1\n"
    "v_cmp_gt_u32 vcc, 4, v246\n"
    "s_setprio 0\n"
    "s_cmp_eq_u64 vcc, 0\n"
    "s_cselect_b32 s34, 10485760, 0\n"
    "v_mfma_f32_32x32x16_bf16 v[0:15], v[128:131], v[96:99], v[0:15]\n"
    "ds_read_b64_tr_b16 v[152:153], v232 offset:8192\n"
    "v_mfma_f32_32x32x16_bf16 v[32:47], v[132:135], v[96:99], v[32:47]\n"
    "ds_read_b64_tr_b16 v[154:155], v232 offset:9216\n"
    "v_mfma_f32_32x32x16_bf16 v[16:31], v[128:131], v[100:103], v[16:31]\n"
    "ds_read_b64_tr_b16 v[156:157], v233 offset:8192\n"
    "buffer_load_dwordx4 v[96:99], v251, s[28:31], s18 offen sc1\n"
    "v_mfma_f32_32x32x16_bf16 v[48:63], v[132:135], v[100:103], v[48:63]\n"
    "ds_read_b64_tr_b16 v[158:159], v233 offset:9216\n"
    "buffer_load_dwordx4 v[100:103], v251, s[28:31], s18 offen offset:1024 sc1\n"
    "s_waitcnt vmcnt(18) lgkmcnt(4)\n"
    "v_mfma_f32_32x32x16_bf16 v[0:15], v[136:139], v[104:107], v[0:15]\n"
    "ds_read_b64_tr_b16 v[128:129], v232 offset:10240\n"
    "v_mfma_f32_32x32x16_bf16 v[32:47], v[140:143], v[104:107], v[32:47]\n"
    "ds_read_b64_tr_b16 v[130:131], v232 offset:11264\n"
    "v_mfma_f32_32x32x16_bf16 v[16:31], v[136:139], v[108:111], v[16:31]\n"
    "ds_read_b64_tr_b16 v[132:133], v233 offset:10240\n"
    "buffer_load_dwordx4 v[104:107], v242, s[32:35], s58 offen sc1\n"
    "v_mfma_f32_32x32x16_bf16 v[48:63], v[140:143], v[108:111], v[48:63]\n"
    "ds_read_b64_tr_b16 v[134:135], v233 offset:11264\n"
    "buffer_load_dwordx4 v[108:111], v242, s[32:35], s58 offen offset:2048 sc1\n"
    "s_waitcnt vmcnt(16) lgkmcnt(4)\n"
    "ds_read_b32 v246, v240 offset:40\n"
    "v_mfma_f32_32x32x16_bf16 v[0:15], v[144:147], v[112:115], v[0:15]\n"
    "ds_read_b64_tr_b16 v[136:137], v232 offset:12288\n"
    "v_mfma_f32_32x32x16_bf16 v[32:47], v[148:151], v[112:115], v[32:47]\n"
    "ds_read_b64_tr_b16 v[138:139], v232 offset:13312\n"
    "v_mfma_f32_32x32x16_bf16 v[16:31], v[144:147], v[116:119], v[16:31]\n"
    "ds_read_b64_tr_b16 v[140:141], v233 offset:12288\n"
    "buffer_load_dwordx4 v[112:115], v242, s[32:35], s59 offen sc1\n"
    "v_mfma_f32_32x32x16_bf16 v[48:63], v[148:151], v[116:119], v[48:63]\n"
    "ds_read_b64_tr_b16 v[142:143], v233 offset:13312\n"
    "buffer_load_dwordx4 v[116:119], v242, s[32:35], s59 offen offset:2048 sc1\n"
    "s_waitcnt vmcnt(14) lgkmcnt(4)\n"
    "s_setprio 1\n"
    "v_cmp_gt_u32 vcc, 4, v246\n"
    "s_setprio 0\n"
    "s_cmp_eq_u64 vcc, 0\n"
    "s_cselect_b32 s38, s34, 0\n"
    "v_mfma_f32_32x32x16_bf16 v[0:15], v[152:155], v[120:123], v[0:15]\n"
    "ds_read_b64_tr_b16 v[144:145], v232 offset:14336\n"
    "v_mfma_f32_32x32x16_bf16 v[32:47], v[156:159], v[120:123], v[32:47]\n"
    "ds_read_b64_tr_b16 v[146:147], v232 offset:15360\n"
    "v_mfma_f32_32x32x16_bf16 v[16:31], v[152:155], v[124:127], v[16:31]\n"
    "ds_read_b64_tr_b16 v[148:149], v233 offset:14336\n"
    "buffer_load_dwordx4 v[120:123], v242, s[32:35], s60 offen sc1\n"
    "v_mfma_f32_32x32x16_bf16 v[48:63], v[156:159], v[124:127], v[48:63]\n"
    "ds_read_b64_tr_b16 v[150:151], v233 offset:15360\n"
    "buffer_load_dwordx4 v[124:127], v242, s[32:35], s60 offen offset:2048 sc1\n"
    "s_waitcnt vmcnt(14) lgkmcnt(4)\n"
    "v_mfma_f32_32x32x16_bf16 v[0:15], v[128:131], v[64:67], v[0:15]\n"
    "ds_read_b64_tr_b16 v[152:153], v238\n"
    "v_mfma_f32_32x32x16_bf16 v[32:47], v[132:135], v[64:67], v[32:47]\n"
    "ds_read_b64_tr_b16 v[154:155], v238 offset:1024\n"
    "v_mfma_f32_32x32x16_bf16 v[16:31], v[128:131], v[68:71], v[16:31]\n"
    "ds_read_b64_tr_b16 v[156:157], v239\n"
    "buffer_load_dwordx4 v[64:67], v242, s[32:35], s61 offen sc1\n"
    "v_mfma_f32_32x32x16_bf16 v[48:63], v[132:135], v[68:71], v[48:63]\n"
    "ds_read_b64_tr_b16 v[158:159], v239 offset:1024\n"
    "buffer_load_dwordx4 v[68:71], v242, s[32:35], s61 offen offset:2048 sc1\n"
    "s_waitcnt vmcnt(14) lgkmcnt(4)\n"
    "v_mfma_f32_32x32x16_bf16 v[0:15], v[136:139], v[72:75], v[0:15]\n"
    "ds_read_b64_tr_b16 v[128:129], v238 offset:2048\n"
    "v_mfma_f32_32x32x16_bf16 v[32:47], v[140:143], v[72:75], v[32:47]\n"
    "ds_read_b64_tr_b16 v[130:131], v238 offset:3072\n"
    "v_mfma_f32_32x32x16_bf16 v[16:31], v[136:139], v[76:79], v[16:31]\n"
    "ds_read_b64_tr_b16 v[132:133], v239 offset:2048\n"
    "buffer_load_dwordx4 v[72:75], v242, s[36:39], s58 offen offset:1024 sc1\n"
    "v_mfma_f32_32x32x16_bf16 v[48:63], v[140:143], v[76:79], v[48:63]\n"
    "ds_read_b64_tr_b16 v[134:135], v239 offset:3072\n"
    "buffer_load_dwordx4 v[76:79], v242, s[36:39], s58 offen offset:3072 sc1\n"
    "s_waitcnt vmcnt(14) lgkmcnt(4)\n"
    "v_mfma_f32_32x32x16_bf16 v[0:15], v[144:147], v[80:83], v[0:15]\n"
    "v_mfma_f32_32x32x16_bf16 v[32:47], v[148:151], v[80:83], v[32:47]\n"
    "v_mfma_f32_32x32x16_bf16 v[16:31], v[144:147], v[84:87], v[16:31]\n"
    "buffer_load_dwordx4 v[80:83], v242, s[36:39], s59 offen offset:1024 sc1\n"
    "v_mfma_f32_32x32x16_bf16 v[48:63], v[148:151], v[84:87], v[48:63]\n"
    "buffer_load_dwordx4 v[84:87], v242, s[36:39], s59 offen offset:3072 sc1\n"
    "s_waitcnt vmcnt(14) lgkmcnt(0)\n"
    "v_mfma_f32_32x32x16_bf16 v[0:15], v[152:155], v[88:91], v[0:15]\n"
    "v_mfma_f32_32x32x16_bf16 v[32:47], v[156:159], v[88:91], v[32:47]\n"
    "v_mfma_f32_32x32x16_bf16 v[16:31], v[152:155], v[92:95], v[16:31]\n"
    "buffer_load_dwordx4 v[88:91], v242, s[36:39], s60 offen offset:1024 sc1\n"
    "v_mfma_f32_32x32x16_bf16 v[48:63], v[156:159], v[92:95], v[48:63]\n"
    "buffer_load_dwordx4 v[92:95], v242, s[36:39], s60 offen offset:3072 sc1\n"
    "s_waitcnt vmcnt(14) lgkmcnt(0)\n"
    "v_mfma_f32_32x32x16_bf16 v[0:15], v[128:131], v[96:99], v[0:15]\n"
    "v_mfma_f32_32x32x16_bf16 v[32:47], v[132:135], v[96:99], v[32:47]\n"
    "v_mfma_f32_32x32x16_bf16 v[16:31], v[128:131], v[100:103], v[16:31]\n"
    "buffer_load_dwordx4 v[96:99], v242, s[36:39], s61 offen offset:1024 sc1\n"
    "v_mfma_f32_32x32x16_bf16 v[48:63], v[132:135], v[100:103], v[48:63]\n"
    "buffer_load_dwordx4 v[100:103], v242, s[36:39], s61 offen offset:3072 sc1\n"
    "s_setprio 1\n"
    "ds_read_b32 v249, v240 offset:40\n"
    "s_cmp_lg_u32 s38, 0\n"
    "s_cbranch_scc1 .Lo_run\n"
    "s_cmp_lg_u32 s34, 0\n"
    "s_cbranch_scc1 .Lo_half\n"
    "ds_read_b32 v246, v240 offset:16\n"
    "s_waitcnt lgkmcnt(0)\n"
    "v_cmp_gt_u32 vcc, 1, v246\n"
    "s_cbranch_vccz .Lpub0_ok\n"
    "s_mov_b32 s71, 8388608\n"
    ".Lpub05437_spin:\n"
    "ds_read_b32 v246, v240 offset:16\n"
    "s_waitcnt lgkmcnt(0)\n"
    "v_cmp_gt_u32 vcc, 1, v246\n"
    "s_cbranch_vccz .Lpub05437_go\n"
    "s_sleep 1\n"
    "s_sub_u32 s71, s71, 1\n"
    "s_cbranch_scc0 .Lpub05437_spin\n"
    ".Lpub05437_go:\n"
    "s_sleep 10\n"
    ".Lpub0_ok:\n"
    "buffer_load_dwordx4 v[104:107], v242, s[28:31], s58 offen sc1\n"
    "buffer_load_dwordx4 v[108:111], v242, s[28:31], s58 offen offset:2048 sc1\n"
    "buffer_load_dwordx4 v[112:115], v242, s[28:31], s59 offen sc1\n"
    "buffer_load_dwordx4 v[116:119], v242, s[28:31], s59 offen offset:2048 sc1\n"
    "buffer_load_dwordx4 v[120:123], v242, s[28:31], s60 offen sc1\n"
    "buffer_load_dwordx4 v[124:127], v242, s[28:31], s60 offen offset:2048 sc1\n"
    "buffer_load_dwordx4 v[64:67], v242, s[28:31], s61 offen sc1\n"
    "buffer_load_dwordx4 v[68:71], v242, s[28:31], s61 offen offset:2048 sc1\n"
    ".Lo_half:\n"
    "s_waitcnt lgkmcnt(0)\n"
    "v_cmp_gt_u32 vcc, 4, v249\n"
    "s_nop 0\n"
    "s_cmp_eq_u64 vcc, 0\n"
    "s_cselect_b32 s38, 10485760, 0\n"
    "buffer_load_dwordx4 v[72:75], v242, s[36:39], s58 offen offset:1024 sc1\n"
    "buffer_load_dwordx4 v[76:79], v242, s[36:39], s58 offen offset:3072 sc1\n"
    "buffer_load_dwordx4 v[80:83], v242, s[36:39], s59 offen offset:1024 sc1\n"
    "buffer_load_dwordx4 v[84:87], v242, s[36:39], s59 offen offset:3072 sc1\n"
    "buffer_load_dwordx4 v[88:91], v242, s[36:39], s60 offen offset:1024 sc1\n"
    "buffer_load_dwordx4 v[92:95], v242, s[36:39], s60 offen offset:3072 sc1\n"
    "buffer_load_dwordx4 v[96:99], v242, s[36:39], s61 offen offset:1024 sc1\n"
    "buffer_load_dwordx4 v[100:103], v242, s[36:39], s61 offen offset:3072 sc1\n"
    ".Lo_run:\n"
    "s_mov_b32 s68, 2097152\n"
    "s_waitcnt vmcnt(14)\n"
    "v_max3_u32 v246, v104, v105, v106\n"
    "v_max3_u32 v247, v107, v108, v109\n"
    "v_max3_u32 v246, v110, v111, v246\n"
    "v_max_u32 v246, v246, v247\n"
    "v_cmp_eq_u32 vcc, -1, v246\n"
    "s_cbranch_vccnz .Lo_re0\n"
    ".Lo_go0:\n"
    "v_mfma_f32_32x32x16_bf16 v[0:15], v[160:163], v[104:107], v[0:15]\n"
    "s_waitcnt vmcnt(12)\n"
    "v_max3_u32 v246, v112, v113, v114\n"
    "v_max3_u32 v247, v115, v116, v117\n"
    "v_mfma_f32_32x32x16_bf16 v[32:47], v[164:167], v[104:107], v[32:47]\n"
    "v_max3_u32 v246, v118, v119, v246\n"
    "v_max_u32 v246, v246, v247\n"
    "v_cmp_eq_u32 vcc, -1, v246\n"
    "v_mfma_f32_32x32x16_bf16 v[0:15], v[168:171], v[108:111], v[0:15]\n"
    "s_cbranch_vccnz .Lo_re2\n"
    ".Lo_go2:\n"
    "v_mfma_f32_32x32x16_bf16 v[32:47], v[172:175], v[108:111], v[32:47]\n"
    "v_mfma_f32_32x32x16_bf16 v[0:15], v[176:179], v[112:115], v[0:15]\n"
    "s_waitcnt vmcnt(10)\n"
    "v_max3_u32 v246, v120, v121, v122\n"
    "v_max3_u32 v247, v123, v124, v125\n"
    "v_mfma_f32_32x32x16_bf16 v[32:47], v[180:183], v[112:115], v[32:47]\n"
    "v_max3_u32 v246, v126, v127, v246\n"
    "v_max_u32 v246, v246, v247\n"
    "v_cmp_eq_u32 vcc, -1, v246\n"
    "v_mfma_f32_32x32x16_bf16 v[0:15], v[184:187], v[116:119], v[0:15]\n"
    "s_cbranch_vccnz .Lo_re4\n"
    ".Lo_go4:\n"
    "v_mfma_f32_32x32x16_bf16 v[32:47], v[188:191], v[116:119], v[32:47]\n"
    "v_mfma_f32_32x32x16_bf16 v[0:15], v[192:195], v[120:123], v[0:15]\n"
    "s_waitcnt vmcnt(8)\n"
    "v_max3_u32 v246, v64, v65, v66\n"
    "v_max3_u32 v247, v67, v68, v69\n"
    "v_mfma_f32_32x32x16_bf16 v[32:47], v[196:199], v[120:123], v[32:47]\n"
    "v_max3_u32 v246, v70, v71, v246\n"
    "v_max_u32 v246, v246, v247\n"
    "v_cmp_eq_u32 vcc, -1, v246\n"
    "v_mfma_f32_32x32x16_bf16 v[0:15], v[200:203], v[124:127], v[0:15]\n"
    "s_cbranch_vccnz .Lo_re6\n"
    ".Lo_go6:\n"
    "v_mfma_f32_32x32x16_bf16 v[32:47], v[204:207], v[124:127], v[32:47]\n"
    "v_mfma_f32_32x32x16_bf16 v[0:15], v[208:211], v[64:67], v[0:15]\n"
    "v_mfma_f32_32x32x16_bf16 v[0:15], v[216:219], v[68:71], v[0:15]\n"
    "v_mfma_f32_32x32x16_bf16 v[32:47], v[212:215], v[64:67], v[32:47]\n"
    "v_mfma_f32_32x32x16_bf16 v[32:47], v[220:223], v[68:71], v[32:47]\n"
    "s_cmp_lg_u32 s38, 0\n"
    "s_cbranch_scc1 .Lo_run1\n"
    "ds_read_b32 v246, v240 offset:40\n"
    "s_waitcnt lgkmcnt(0)\n"
    "v_cmp_gt_u32 vcc, 1, v246\n"
    "s_cbranch_vccz .Lpub1_ok\n"
    "s_mov_b32 s71, 8388608\n"
    ".Lpub15527_spin:\n"
    "ds_read_b32 v246, v240 offset:40\n"
    "s_waitcnt lgkmcnt(0)\n"
    "v_cmp_gt_u32 vcc, 1, v246\n"
    "s_cbranch_vccz .Lpub15527_go\n"
    "s_sleep 1\n"
    "s_sub_u32 s71, s71, 1\n"
    "s_cbranch_scc0 .Lpub15527_spin\n"
    ".Lpub15527_go:\n"
    "s_sleep 10\n"
    ".Lpub1_ok:\n"
    "buffer_load_dwordx4 v[72:75], v242, s[28:31], s58 offen offset:1024 sc1\n"
    "buffer_load_dwordx4 v[76:79], v242, s[28:31], s58 offen offset:3072 sc1\n"
    "buffer_load_dwordx4 v[80:83], v242, s[28:31], s59 offen offset:1024 sc1\n"
    "buffer_load_dwordx4 v[84:87], v242, s[28:31], s59 offen offset:3072 sc1\n"
    "buffer_load_dwordx4 v[88:91], v242, s[28:31], s60 offen offset:1024 sc1\n"
    "buffer_load_dwordx4 v[92:95], v242, s[28:31], s60 offen offset:3072 sc1\n"
    "buffer_load_dwordx4 v[96:99], v242, s[28:31], s61 offen offset:1024 sc1\n"
    "buffer_load_dwordx4 v[100:103], v242, s[28:31], s61 offen offset:3072 sc1\n"
    ".Lo_run1:\n"
    "s_waitcnt vmcnt(6)\n"
    "v_max3_u32 v246, v72, v73, v74\n"
    "v_max3_u32 v247, v75, v76, v77\n"
    "v_max3_u32 v246, v78, v79, v246\n"
    "v_max_u32 v246, v246, v247\n"
    "v_cmp_eq_u32 vcc, -1, v246\n"
    "s_cbranch_vccnz .Lo_re8\n"
    ".Lo_go8:\n"
    "v_mfma_f32_32x32x16_bf16 v[16:31], v[160:163], v[72:75], v[16:31]\n"
    "s_waitcnt vmcnt(4)\n"
    "v_max3_u32 v246, v80, v81, v82\n"
    "v_max3_u32 v247, v83, v84, v85\n"
    "v_mfma_f32_32x32x16_bf16 v[48:63], v[164:167], v[72:75], v[48:63]\n"
    "v_max3_u32 v246, v86, v87, v246\n"
    "v_max_u32 v246, v246, v247\n"
    "v_cmp_eq_u32 vcc, -1, v246\n"
    "v_mfma_f32_32x32x16_bf16 v[16:31], v[168:171], v[76:79], v[16:31]\n"
    "s_cbranch_vccnz .Lo_re10\n"
    ".Lo_go10:\n"
    "v_mfma_f32_32x32x16_bf16 v[48:63], v[172:175], v[76:79], v[48:63]\n"
    "ds_write_b128 v228, v[0:3]\n"
    "v_mfma_f32_32x32x16_bf16 v[16:31], v[176:179], v[80:83], v[16:31]\n"
    "s_waitcnt vmcnt(2)\n"
    "v_max3_u32 v246, v88, v89, v90\n"
    "v_max3_u32 v247, v91, v92, v93\n"
    "v_mfma_f32_32x32x16_bf16 v[48:63], v[180:183], v[80:83], v[48:63]\n"
    "v_max3_u32 v246, v94, v95, v246\n"
    "v_max_u32 v246, v246, v247\n"
    "v_cmp_eq_u32 vcc, -1, v246\n"
    "v_mfma_f32_32x32x16_bf16 v[16:31], v[184:187], v[84:87], v[16:31]\n"
    "s_cbranch_vccnz .Lo_re12\n"
    ".Lo_go12:\n"
    "ds_write_b128 v229, v[4:7]\n"
    "v_mfma_f32_32x32x16_bf16 v[48:63], v[188:191], v[84:87], v[48:63]\n"
    "ds_write_b128 v230, v[8:11]\n"
    "v_mfma_f32_32x32x16_bf16 v[16:31], v[192:195], v[88:91], v[16:31]\n"
    "s_waitcnt vmcnt(0)\n"
    "v_max3_u32 v246, v96, v97, v98\n"
    "v_max3_u32 v247, v99, v100, v101\n"
    "v_mfma_f32_32x32x16_bf16 v[48:63], v[196:199], v[88:91], v[48:63]\n"
    "v_max3_u32 v246, v102, v103, v246\n"
    "v_max_u32 v246, v246, v247\n"
    "v_cmp_eq_u32 vcc, -1, v246\n"
    "v_mfma_f32_32x32x16_bf16 v[16:31], v[200:203], v[92:95], v[16:31]\n"
    "s_cbranch_vccnz .Lo_re14\n"
    ".Lo_go14:\n"
    "ds_write_b128 v231, v[12:15]\n"
    "v_mfma_f32_32x32x16_bf16 v[48:63], v[204:207], v[92:95], v[48:63]\n"
    "ds_write_b128 v228, v[32:35] offset:128\n"
    "v_mfma_f32_32x32x16_bf16 v[16:31], v[208:211], v[96:99], v[16:31]\n"
    "ds_write_b128 v229, v[36:39] offset:128\n"
    "v_mfma_f32_32x32x16_bf16 v[16:31], v[216:219], v[100:103], v[16:31]\n"
    "ds_write_b128 v230, v[40:43] offset:128\n"
    "v_mfma_f32_32x32x16_bf16 v[48:63], v[212:215], v[96:99], v[48:63]\n"
    "ds_write_b128 v231, v[44:47] offset:128\n"
    "v_mfma_f32_32x32x16_bf16 v[48:63], v[220:223], v[100:103], v[48:63]\n"
    "s_nop 7\n"
    "ds_write_b128 v228, v[16:19] offset:8192\n"
    "ds_write_b128 v229, v[20:23] offset:8192\n"
    "ds_write_b128 v230, v[24:27] offset:8192\n"
    "ds_write_b128 v231, v[28:31] offset:8192\n"
    "ds_write_b128 v228, v[48:51] offset:8320\n"
    "ds_write_b128 v229, v[52:55] offset:8320\n"
    "ds_write_b128 v230, v[56:59] offset:8320\n"
    "ds_write_b128 v231, v[60:63] offset:8320\n"
    ".Lfinal:\n"
    "v_lshrrev_b32 v178, 3, v243\n"
    "s_lshl_b32 s58, s54, 5\n"
    "s_add_u32 s58, s58, 162048\n"
    "v_lshl_add_u32 v181, v178, 2, s58\n"
    "ds_read_b32 v176, v181\n"
    "v_and_b32 v179, 7, v243\n"
    "v_lshlrev_b32 v179, 1, v179\n"
    "v_xor_b32 v179, v179, v178\n"
    "v_lshlrev_b32 v179, 4, v179\n"
    "v_lshl_add_u32 v179, v178, 8, v179\n"
    "s_lshl_b32 s58, s54, 11\n"
    "s_add_u32 s58, s58, 65536\n"
    "v_add_u32 v179, s58, v179\n"
    "v_xor_b32 v180, 16, v179\n"
    "v_lshlrev_b32 v160, 16, v252\n"
    "v_and_b32 v161, 0xffff0000, v252\n"
    "v_lshlrev_b32 v162, 16, v253\n"
    "v_and_b32 v163, 0xffff0000, v253\n"
    "v_lshlrev_b32 v164, 16, v254\n"
    "v_and_b32 v165, 0xffff0000, v254\n"
    "v_lshlrev_b32 v166, 16, v255\n"
    "v_and_b32 v167, 0xffff0000, v255\n"
    "s_waitcnt lgkmcnt(0)\n"
    "v_rcp_f32 v176, v176\n"
    "s_barrier\n"
    "ds_read_b128 v[128:131], v179\n"
    "ds_read_b128 v[132:135], v180\n"
    "ds_read_b128 v[136:139], v179 offset:16384\n"
    "ds_read_b128 v[140:143], v180 offset:16384\n"
    "ds_read_b128 v[144:147], v179 offset:32768\n"
    "ds_read_b128 v[148:151], v180 offset:32768\n"
    "ds_read_b128 v[152:155], v179 offset:49152\n"
    "ds_read_b128 v[156:159], v180 offset:49152\n"
    "s_waitcnt lgkmcnt(4)\n"
    "v_pk_add_f32 v[128:129], v[128:129], v[136:137]\n"
    "v_pk_add_f32 v[130:131], v[130:131], v[138:139]\n"
    "v_pk_add_f32 v[132:133], v[132:133], v[140:141]\n"
    "v_pk_add_f32 v[134:135], v[134:135], v[142:143]\n"
    "s_waitcnt lgkmcnt(2)\n"
    "v_pk_add_f32 v[128:129], v[128:129], v[144:145]\n"
    "v_pk_add_f32 v[130:131], v[130:131], v[146:147]\n"
    "v_pk_add_f32 v[132:133], v[132:133], v[148:149]\n"
    "v_pk_add_f32 v[134:135], v[134:135], v[150:151]\n"
    "s_waitcnt lgkmcnt(0)\n"
    "v_pk_add_f32 v[128:129], v[128:129], v[152:153]\n"
    "v_pk_add_f32 v[130:131], v[130:131], v[154:155]\n"
    "v_pk_add_f32 v[132:133], v[132:133], v[156:157]\n"
    "v_pk_add_f32 v[134:135], v[134:135], v[158:159]\n"
    "v_fmac_f32 v160, v176, v128\n"
    "v_fmac_f32 v161, v176, v129\n"
    "v_fmac_f32 v162, v176, v130\n"
    "v_fmac_f32 v163, v176, v131\n"
    "v_fmac_f32 v164, v176, v132\n"
    "v_fmac_f32 v165, v176, v133\n"
    "v_fmac_f32 v166, v176, v134\n"
    "v_fmac_f32 v167, v176, v135\n"
    "v_cvt_pk_bf16_f32 v128, v160, v161\n"
    "v_cvt_pk_bf16_f32 v129, v162, v163\n"
    "v_cvt_pk_bf16_f32 v130, v164, v165\n"
    "v_cvt_pk_bf16_f32 v131, v166, v167\n"
    "buffer_store_dwordx4 v[128:131], v244, s[48:51], 0 offen sc1\n"
    "s_branch .Lend\n"
    ".Lo_re0:\n"
    "s_cmp_eq_u32 s68, 0\n"
    "s_cbranch_scc1 .Lo_go0\n"
    "s_sub_u32 s68, s68, 1\n"
    "s_sleep 1\n"
    "buffer_load_dwordx4 v[104:107], v242, s[28:31], s58 offen sc1\n"
    "buffer_load_dwordx4 v[108:111], v242, s[28:31], s58 offen offset:2048 sc1\n"
    "s_waitcnt vmcnt(0)\n"
    "s_waitcnt vmcnt(14)\n"
    "v_max3_u32 v246, v104, v105, v106\n"
    "v_max3_u32 v247, v107, v108, v109\n"
    "v_max3_u32 v246, v110, v111, v246\n"
    "v_max_u32 v246, v246, v247\n"
    "v_cmp_eq_u32 vcc, -1, v246\n"
    "s_cbranch_vccnz .Lo_re0\n"
    "buffer_load_dwordx4 v[112:115], v242, s[28:31], s59 offen sc1\n"
    "buffer_load_dwordx4 v[116:119], v242, s[28:31], s59 offen offset:2048 sc1\n"
    "buffer_load_dwordx4 v[120:123], v242, s[28:31], s60 offen sc1\n"
    "buffer_load_dwordx4 v[124:127], v242, s[28:31], s60 offen offset:2048 sc1\n"
    "buffer_load_dwordx4 v[64:67], v242, s[28:31], s61 offen sc1\n"
    "buffer_load_dwordx4 v[68:71], v242, s[28:31], s61 offen offset:2048 sc1\n"
    "buffer_load_dwordx4 v[72:75], v242, s[36:39], s58 offen offset:1024 sc1\n"
    "buffer_load_dwordx4 v[76:79], v242, s[36:39], s58 offen offset:3072 sc1\n"
    "buffer_load_dwordx4 v[80:83], v242, s[36:39], s59 offen offset:1024 sc1\n"
    "buffer_load_dwordx4 v[84:87], v242, s[36:39], s59 offen offset:3072 sc1\n"
    "buffer_load_dwordx4 v[88:91], v242, s[36:39], s60 offen offset:1024 sc1\n"
    "buffer_load_dwordx4 v[92:95], v242, s[36:39], s60 offen offset:3072 sc1\n"
    "buffer_load_dwordx4 v[96:99], v242, s[36:39], s61 offen offset:1024 sc1\n"
    "buffer_load_dwordx4 v[100:103], v242, s[36:39], s61 offen offset:3072 sc1\n"
    "s_branch .Lo_go0\n"
    ".Lo_re2:\n"
    "s_cmp_eq_u32 s68, 0\n"
    "s_cbranch_scc1 .Lo_go2\n"
    "s_sub_u32 s68, s68, 1\n"
    "s_sleep 1\n"
    "buffer_load_dwordx4 v[112:115], v242, s[28:31], s59 offen sc1\n"
    "buffer_load_dwordx4 v[116:119], v242, s[28:31], s59 offen offset:2048 sc1\n"
    "s_waitcnt vmcnt(0)\n"
    "s_waitcnt vmcnt(12)\n"
    "v_max3_u32 v246, v112, v113, v114\n"
    "v_max3_u32 v247, v115, v116, v117\n"
    "v_max3_u32 v246, v118, v119, v246\n"
    "v_max_u32 v246, v246, v247\n"
    "v_cmp_eq_u32 vcc, -1, v246\n"
    "s_cbranch_vccnz .Lo_re2\n"
    "buffer_load_dwordx4 v[120:123], v242, s[28:31], s60 offen sc1\n"
    "buffer_load_dwordx4 v[124:127], v242, s[28:31], s60 offen offset:2048 sc1\n"
    "buffer_load_dwordx4 v[64:67], v242, s[28:31], s61 offen sc1\n"
    "buffer_load_dwordx4 v[68:71], v242, s[28:31], s61 offen offset:2048 sc1\n"
    "buffer_load_dwordx4 v[72:75], v242, s[36:39], s58 offen offset:1024 sc1\n"
    "buffer_load_dwordx4 v[76:79], v242, s[36:39], s58 offen offset:3072 sc1\n"
    "buffer_load_dwordx4 v[80:83], v242, s[36:39], s59 offen offset:1024 sc1\n"
    "buffer_load_dwordx4 v[84:87], v242, s[36:39], s59 offen offset:3072 sc1\n"
    "buffer_load_dwordx4 v[88:91], v242, s[36:39], s60 offen offset:1024 sc1\n"
    "buffer_load_dwordx4 v[92:95], v242, s[36:39], s60 offen offset:3072 sc1\n"
    "buffer_load_dwordx4 v[96:99], v242, s[36:39], s61 offen offset:1024 sc1\n"
    "buffer_load_dwordx4 v[100:103], v242, s[36:39], s61 offen offset:3072 sc1\n"
    "s_branch .Lo_go2\n"
    ".Lo_re4:\n"
    "s_cmp_eq_u32 s68, 0\n"
    "s_cbranch_scc1 .Lo_go4\n"
    "s_sub_u32 s68, s68, 1\n"
    "s_sleep 1\n"
    "buffer_load_dwordx4 v[120:123], v242, s[28:31], s60 offen sc1\n"
    "buffer_load_dwordx4 v[124:127], v242, s[28:31], s60 offen offset:2048 sc1\n"
    "s_waitcnt vmcnt(0)\n"
    "s_waitcnt vmcnt(10)\n"
    "v_max3_u32 v246, v120, v121, v122\n"
    "v_max3_u32 v247, v123, v124, v125\n"
    "v_max3_u32 v246, v126, v127, v246\n"
    "v_max_u32 v246, v246, v247\n"
    "v_cmp_eq_u32 vcc, -1, v246\n"
    "s_cbranch_vccnz .Lo_re4\n"
    "buffer_load_dwordx4 v[64:67], v242, s[28:31], s61 offen sc1\n"
    "buffer_load_dwordx4 v[68:71], v242, s[28:31], s61 offen offset:2048 sc1\n"
    "buffer_load_dwordx4 v[72:75], v242, s[36:39], s58 offen offset:1024 sc1\n"
    "buffer_load_dwordx4 v[76:79], v242, s[36:39], s58 offen offset:3072 sc1\n"
    "buffer_load_dwordx4 v[80:83], v242, s[36:39], s59 offen offset:1024 sc1\n"
    "buffer_load_dwordx4 v[84:87], v242, s[36:39], s59 offen offset:3072 sc1\n"
    "buffer_load_dwordx4 v[88:91], v242, s[36:39], s60 offen offset:1024 sc1\n"
    "buffer_load_dwordx4 v[92:95], v242, s[36:39], s60 offen offset:3072 sc1\n"
    "buffer_load_dwordx4 v[96:99], v242, s[36:39], s61 offen offset:1024 sc1\n"
    "buffer_load_dwordx4 v[100:103], v242, s[36:39], s61 offen offset:3072 sc1\n"
    "s_branch .Lo_go4\n"
    ".Lo_re6:\n"
    "s_cmp_eq_u32 s68, 0\n"
    "s_cbranch_scc1 .Lo_go6\n"
    "s_sub_u32 s68, s68, 1\n"
    "s_sleep 1\n"
    "buffer_load_dwordx4 v[64:67], v242, s[28:31], s61 offen sc1\n"
    "buffer_load_dwordx4 v[68:71], v242, s[28:31], s61 offen offset:2048 sc1\n"
    "s_waitcnt vmcnt(0)\n"
    "s_waitcnt vmcnt(8)\n"
    "v_max3_u32 v246, v64, v65, v66\n"
    "v_max3_u32 v247, v67, v68, v69\n"
    "v_max3_u32 v246, v70, v71, v246\n"
    "v_max_u32 v246, v246, v247\n"
    "v_cmp_eq_u32 vcc, -1, v246\n"
    "s_cbranch_vccnz .Lo_re6\n"
    "buffer_load_dwordx4 v[72:75], v242, s[36:39], s58 offen offset:1024 sc1\n"
    "buffer_load_dwordx4 v[76:79], v242, s[36:39], s58 offen offset:3072 sc1\n"
    "buffer_load_dwordx4 v[80:83], v242, s[36:39], s59 offen offset:1024 sc1\n"
    "buffer_load_dwordx4 v[84:87], v242, s[36:39], s59 offen offset:3072 sc1\n"
    "buffer_load_dwordx4 v[88:91], v242, s[36:39], s60 offen offset:1024 sc1\n"
    "buffer_load_dwordx4 v[92:95], v242, s[36:39], s60 offen offset:3072 sc1\n"
    "buffer_load_dwordx4 v[96:99], v242, s[36:39], s61 offen offset:1024 sc1\n"
    "buffer_load_dwordx4 v[100:103], v242, s[36:39], s61 offen offset:3072 sc1\n"
    "s_branch .Lo_go6\n"
    ".Lo_re8:\n"
    "s_cmp_eq_u32 s68, 0\n"
    "s_cbranch_scc1 .Lo_go8\n"
    "s_sub_u32 s68, s68, 1\n"
    "s_sleep 1\n"
    "buffer_load_dwordx4 v[72:75], v242, s[28:31], s58 offen offset:1024 sc1\n"
    "buffer_load_dwordx4 v[76:79], v242, s[28:31], s58 offen offset:3072 sc1\n"
    "s_waitcnt vmcnt(0)\n"
    "s_waitcnt vmcnt(6)\n"
    "v_max3_u32 v246, v72, v73, v74\n"
    "v_max3_u32 v247, v75, v76, v77\n"
    "v_max3_u32 v246, v78, v79, v246\n"
    "v_max_u32 v246, v246, v247\n"
    "v_cmp_eq_u32 vcc, -1, v246\n"
    "s_cbranch_vccnz .Lo_re8\n"
    "buffer_load_dwordx4 v[80:83], v242, s[28:31], s59 offen offset:1024 sc1\n"
    "buffer_load_dwordx4 v[84:87], v242, s[28:31], s59 offen offset:3072 sc1\n"
    "buffer_load_dwordx4 v[88:91], v242, s[28:31], s60 offen offset:1024 sc1\n"
    "buffer_load_dwordx4 v[92:95], v242, s[28:31], s60 offen offset:3072 sc1\n"
    "buffer_load_dwordx4 v[96:99], v242, s[28:31], s61 offen offset:1024 sc1\n"
    "buffer_load_dwordx4 v[100:103], v242, s[28:31], s61 offen offset:3072 sc1\n"
    "s_branch .Lo_go8\n"
    ".Lo_re10:\n"
    "s_cmp_eq_u32 s68, 0\n"
    "s_cbranch_scc1 .Lo_go10\n"
    "s_sub_u32 s68, s68, 1\n"
    "s_sleep 1\n"
    "buffer_load_dwordx4 v[80:83], v242, s[28:31], s59 offen offset:1024 sc1\n"
    "buffer_load_dwordx4 v[84:87], v242, s[28:31], s59 offen offset:3072 sc1\n"
    "s_waitcnt vmcnt(0)\n"
    "s_waitcnt vmcnt(4)\n"
    "v_max3_u32 v246, v80, v81, v82\n"
    "v_max3_u32 v247, v83, v84, v85\n"
    "v_max3_u32 v246, v86, v87, v246\n"
    "v_max_u32 v246, v246, v247\n"
    "v_cmp_eq_u32 vcc, -1, v246\n"
    "s_cbranch_vccnz .Lo_re10\n"
    "buffer_load_dwordx4 v[88:91], v242, s[28:31], s60 offen offset:1024 sc1\n"
    "buffer_load_dwordx4 v[92:95], v242, s[28:31], s60 offen offset:3072 sc1\n"
    "buffer_load_dwordx4 v[96:99], v242, s[28:31], s61 offen offset:1024 sc1\n"
    "buffer_load_dwordx4 v[100:103], v242, s[28:31], s61 offen offset:3072 sc1\n"
    "s_branch .Lo_go10\n"
    ".Lo_re12:\n"
    "s_cmp_eq_u32 s68, 0\n"
    "s_cbranch_scc1 .Lo_go12\n"
    "s_sub_u32 s68, s68, 1\n"
    "s_sleep 1\n"
    "buffer_load_dwordx4 v[88:91], v242, s[28:31], s60 offen offset:1024 sc1\n"
    "buffer_load_dwordx4 v[92:95], v242, s[28:31], s60 offen offset:3072 sc1\n"
    "s_waitcnt vmcnt(0)\n"
    "s_waitcnt vmcnt(2)\n"
    "v_max3_u32 v246, v88, v89, v90\n"
    "v_max3_u32 v247, v91, v92, v93\n"
    "v_max3_u32 v246, v94, v95, v246\n"
    "v_max_u32 v246, v246, v247\n"
    "v_cmp_eq_u32 vcc, -1, v246\n"
    "s_cbranch_vccnz .Lo_re12\n"
    "buffer_load_dwordx4 v[96:99], v242, s[28:31], s61 offen offset:1024 sc1\n"
    "buffer_load_dwordx4 v[100:103], v242, s[28:31], s61 offen offset:3072 sc1\n"
    "s_branch .Lo_go12\n"
    ".Lo_re14:\n"
    "s_cmp_eq_u32 s68, 0\n"
    "s_cbranch_scc1 .Lo_go14\n"
    "s_sub_u32 s68, s68, 1\n"
    "s_sleep 1\n"
    "buffer_load_dwordx4 v[96:99], v242, s[28:31], s61 offen offset:1024 sc1\n"
    "buffer_load_dwordx4 v[100:103], v242, s[28:31], s61 offen offset:3072 sc1\n"
    "s_waitcnt vmcnt(0)\n"
    "s_waitcnt vmcnt(0)\n"
    "v_max3_u32 v246, v96, v97, v98\n"
    "v_max3_u32 v247, v99, v100, v101\n"
    "v_max3_u32 v246, v102, v103, v246\n"
    "v_max_u32 v246, v246, v247\n"
    "v_cmp_eq_u32 vcc, -1, v246\n"
    "s_cbranch_vccnz .Lo_re14\n"
    "s_branch .Lo_go14\n"
    ".Lend:\n"
    : "+v"(tid)
    : "s"(__builtin_amdgcn_kernarg_segment_ptr()), "s"(blockIdx.x),
      "s"((unsigned)(size_t)(__attribute__((address_space(3))) char*)lds)
    : "v1", "v2", "v3", "v4", "v5", "v6", "v7", "v8", "v9", "v10", "v11", "v12", "v13", "v14", "v15", "v16", "v17", "v18", "v19", "v20", "v21", "v22", "v23", "v24", "v25", "v26", "v27", "v28", "v29", "v30", "v31", "v32", "v33", "v34", "v35", "v36", "v37", "v38", "v39", "v40", "v41", "v42", "v43", "v44", "v45", "v46", "v47", "v48", "v49", "v50", "v51", "v52", "v53", "v54", "v55", "v56", "v57", "v58", "v59", "v60", "v61", "v62", "v63", "v64", "v65", "v66", "v67", "v68", "v69", "v70", "v71", "v72", "v73", "v74", "v75", "v76", "v77", "v78", "v79", "v80", "v81", "v82", "v83", "v84", "v85", "v86", "v87", "v88", "v89", "v90", "v91", "v92", "v93", "v94", "v95", "v96", "v97", "v98", "v99", "v100", "v101", "v102", "v103", "v104", "v105", "v106", "v107", "v108", "v109", "v110", "v111", "v112", "v113", "v114", "v115", "v116", "v117", "v118", "v119", "v120", "v121", "v122", "v123", "v124", "v125", "v126", "v127", "v128", "v129", "v130", "v131", "v132", "v133", "v134", "v135", "v136", "v137", "v138", "v139", "v140", "v141", "v142", "v143", "v144", "v145", "v146", "v147", "v148", "v149", "v150", "v151", "v152", "v153", "v154", "v155", "v156", "v157", "v158", "v159", "v160", "v161", "v162", "v163", "v164", "v165", "v166", "v167", "v168", "v169", "v170", "v171", "v172", "v173", "v174", "v175", "v176", "v177", "v178", "v179", "v180", "v181", "v182", "v183", "v184", "v185", "v186", "v187", "v188", "v189", "v190", "v191", "v192", "v193", "v194", "v195", "v196", "v197", "v198", "v199", "v200", "v201", "v202", "v203", "v204", "v205", "v206", "v207", "v208", "v209", "v210", "v211", "v212", "v213", "v214", "v215", "v216", "v217", "v218", "v219", "v220", "v221", "v222", "v223", "v224", "v225", "v226", "v227", "v228", "v229", "v230", "v231", "v232", "v233", "v234", "v235", "v236", "v237", "v238", "v239", "v240", "v241", "v242", "v243", "v244", "v245", "v246", "v247", "v248", "v249", "v250", "v251", "v252", "v253", "v254", "v255", "s4", "s5", "s6", "s7", "s8", "s9", "s10", "s11", "s12", "s13", "s14", "s15", "s16", "s17", "s18", "s19", "s20", "s21", "s22", "s23", "s24", "s25", "s26", "s27", "s28", "s29", "s30", "s31", "s32", "s33", "s34", "s35", "s36", "s37", "s38", "s39", "s40", "s41", "s42", "s43", "s44", "s45", "s46", "s47", "s48", "s49", "s50", "s51", "s52", "s53", "s54", "s55", "s56", "s57", "s58", "s59", "s60", "s61", "s62", "s63", "s64", "s65", "s66", "s67", "s68", "s69", "s70", "s71", "s72", "s73", "s74", "s75", "s76", "s77", "s78", "s79", "s80", "s81", "s82", "s83", "s84", "s85", "s86", "s87", "s88", "s89", "s90", "s91", "s92", "s93", "s94", "s95", "s96", "s97", "s98", "s99", "s100", "s101", "vcc", "scc", "memory");
}

extern "C" void kernel_launch(void* const* d_in, const int* in_sizes, int n_in, void* d_out, int out_size,
                              void* d_ws, size_t ws_size, hipStream_t stream) {
  fused_ln_mqa_reglu<<<256, 512, 0, stream>>>(d_in[0], d_in[1], d_in[2], d_in[3], d_in[4], d_in[6], d_in[7],
                                              d_in[8], d_out, d_ws);
}
```
